```python
import torch

def _softmax(x, axis=-1):
    return torch.softmax(x, dim=axis)
def _gelu(x):
    return torch.nn.functional.gelu(x, approximate="tanh")
def _silu(x):
    return torch.nn.functional.silu(x)
def _sigmoid(x):
    return torch.sigmoid(x)
def _layernorm(x, axis=-1, eps=1e-5):
    m = x.mean(dim=axis, keepdim=True)
    v = x.var(dim=axis, keepdim=True, unbiased=False)
    return (x - m) / torch.sqrt(v + eps)
def _scatter_add(tgt, idx, src, axis=0):
    return tgt.clone().index_add_(axis, idx, src)
def _segment_sum(src, seg, n):
    out = torch.zeros((n,) + src.shape[1:], dtype=src.dtype)
    return out.index_add_(0, seg, src)
def _pad(x, pw):
    flat = tuple(v for pair in reversed(pw) for v in pair)
    return torch.nn.functional.pad(x, flat)

kernel_name = 'interleave_layers__linear_attn_block__glu_gated_bilinear__prompt'

def setup_inputs():

    _w = (lambda a: (lambda t: t.to(torch.bfloat16) if t.is_floating_point() else t)(torch.as_tensor(a)))
    def _A_setup():
        B, S, D = 1, 2048, 1024
        return {
            "x": _w((lambda *s: torch.randn(*s, dtype=torch.bfloat16) * 0.05)(B, S, D)),
            "wqkv": _w((lambda *s: torch.randn(*s, dtype=torch.bfloat16) * 0.05)(D, 3 * D)),
            "wo": _w((lambda *s: torch.randn(*s, dtype=torch.bfloat16) * 0.05)(D, D)),
        }
    _A_in = _A_setup()

    def _B_setup():
        B, S, D, F = 1, 2048, 1024, 3584
        return {
            "x": _w((lambda *s: torch.randn(*s, dtype=torch.bfloat16) * 0.05)(B, S, D)),
            "w_gate": _w((lambda *s: torch.randn(*s, dtype=torch.bfloat16) * 0.8)(D, F) * (D ** -0.5)),
            "w_up": _w((lambda *s: torch.randn(*s, dtype=torch.bfloat16) * 0.8)(D, F) * (D ** -0.5)),
            "w_down": _w((lambda *s: torch.randn(*s, dtype=torch.bfloat16) * 0.8)(F, D) * (F ** -0.5)),
        }
    _B_in = _B_setup()

    return {
        "A_x": _A_in["x"],
        "A_wqkv": _A_in["wqkv"],
        "A_wo": _A_in["wo"],
        "B_x": _B_in["x"],
        "B_w_gate": _B_in["w_gate"],
        "B_w_up": _B_in["w_up"],
        "B_w_down": _B_in["w_down"],
    }

def reference(A_x, A_wqkv, A_wo, B_x, B_w_gate, B_w_up, B_w_down):
    def _A_ref(x, wqkv, wo):
        B, S, D = x.shape
        H = 32; Dh = D // H
        qkv = (lambda x, *d: x.permute(*d))((x @ wqkv).reshape(B, S, 3, H, Dh), 2, 0, 3, 1, 4)
        q, k, v = qkv[0], qkv[1], qkv[2]
        phi = lambda t: torch.where(t > 0, t + 1, torch.exp(t)) + 1e-6
        pq, pk = phi(q), phi(k)
        kv = torch.einsum("bhsd,bhse->bhde", pk, v)
        z = torch.einsum("bhsd->bhd", pk)
        num = torch.einsum("bhqd,bhde->bhqe", pq, kv)
        den = torch.einsum("bhqd,bhd->bhq", pq, z)[..., None] + 1e-6
        o = num / den
        return (lambda x, *d: x.permute(*d))(o, 0, 2, 1, 3).reshape(B, S, D) @ wo

    def _B_ref(x, w_gate, w_up, w_down):
        g = torch.einsum("bsd,df->bsf", x, w_gate)
        u = torch.einsum("bsd,df->bsf", x, w_up)
        h = g * u
        return torch.einsum("bsf,fd->bsd", h, w_down)

    x = A_x
    x = x + _A_ref(_layernorm(x, axis=-1), A_wqkv, A_wo)
    x = x + _B_ref(_layernorm(x, axis=-1), B_w_gate, B_w_up, B_w_down)
    x = x + _A_ref(_layernorm(x, axis=-1), A_wqkv, A_wo)
    x = x + _B_ref(_layernorm(x, axis=-1), B_w_gate, B_w_up, B_w_down)
    return x
```

```cpp
#include <hip/hip_runtime.h>
#include <hip/hip_bf16.h>
#include <stdint.h>

#define LAS __attribute__((address_space(3)))
#define DI __device__ __forceinline__
typedef unsigned short u16;
typedef __bf16 bf16x8 __attribute__((ext_vector_type(8)));
typedef __bf16 bf16x2 __attribute__((ext_vector_type(2)));
typedef short s16x4 __attribute__((ext_vector_type(4)));
typedef short s16x8 __attribute__((ext_vector_type(8)));
typedef float f32x2 __attribute__((ext_vector_type(2)));
typedef float f32x4 __attribute__((ext_vector_type(4)));
typedef unsigned u32x2 __attribute__((ext_vector_type(2)));
typedef unsigned u32x4 __attribute__((ext_vector_type(4)));
typedef u32x4 rsrc_t;

constexpr int S_ = 2048, D_ = 1024, F_ = 3584;
constexpr unsigned OOB_V = 0x80000000u, OOB_S = 0x7f000000u;

DI rsrc_t mk_rsrc(const void* p, unsigned bytes) {
  const unsigned long long a = (unsigned long long)p;
  rsrc_t r = {(unsigned)a, (unsigned)(a >> 32) & 0xffffu, bytes, 0x00020000u};
  return r;
}
DI void dma16(rsrc_t r, LAS char* dst, unsigned voff, unsigned soff) {
  asm volatile("s_mov_b32 m0, %0\n\ts_nop 0\n\tbuffer_load_dwordx4 %1, %2, %3 offen lds" :: "s"((unsigned)(size_t)dst), "v"(voff), "s"(r), "s"(soff) : "memory");
}
DI f32x4 mfma(bf16x8 a, bf16x8 b, f32x4 c) { return __builtin_amdgcn_mfma_f32_16x16x32_bf16(a, b, c, 0, 0, 0); }
DI bf16x8 ld128(LAS char* p) { return *(LAS bf16x8*)p; }
DI bf16x8 ldtr(LAS char* p, int off2) {
  s16x4 a = __builtin_amdgcn_ds_read_tr16_b64_v4i16((LAS s16x4*)p);
  s16x4 b = __builtin_amdgcn_ds_read_tr16_b64_v4i16((LAS s16x4*)(p + off2));
  return __builtin_bit_cast(bf16x8, __builtin_shufflevector(a, b, 0, 1, 2, 3, 4, 5, 6, 7));
}
DI unsigned pk2(float a, float b) { f32x2 v = {a, b}; return __builtin_bit_cast(unsigned, __builtin_convertvector(v, bf16x2)); }
DI float bflo(unsigned u) { return __uint_as_float(u << 16); }
DI float bfhi(unsigned u) { return __uint_as_float(u & 0xffff0000u); }
DI float phi(float t) { return (t > 0.f ? t + 1.f : __expf(t)) + 1e-6f; }
#define FENCE() asm volatile("" ::: "memory")
#define BAR() do { FENCE(); __builtin_amdgcn_s_barrier(); FENCE(); __builtin_amdgcn_sched_barrier(0); } while (0)
#define WAITV(n) asm volatile("s_waitcnt vmcnt(%0)" :: "i"(n) : "memory")
#define WAITL0() asm volatile("s_waitcnt lgkmcnt(0)" ::: "memory")
#define GLOAD4(dst, ptr, off) asm volatile("global_load_dwordx4 %0, %1, off offset:%2" : "=v"(dst) : "v"(ptr), "i"(off) : "memory")

DI f32x2 combine_stats(const f32x4 (&v)[4]) {
  float s = (v[0][0] + v[0][2]) + (v[1][0] + v[1][2]) + (v[2][0] + v[2][2]) + (v[3][0] + v[3][2]);
  s += __shfl_xor(s, 1);
  const float mean = s * (1.f / 1024.f);
  float q = 0.f;
#pragma unroll
  for (int i = 0; i < 4; ++i) {
    const float d0 = v[i][0] * (1.f / 64.f) - mean, d1 = v[i][2] * (1.f / 64.f) - mean;
    q += v[i][1] + v[i][3] + 64.f * (d0 * d0 + d1 * d1);
  }
  q += __shfl_xor(q, 1);
  f32x2 r; r[0] = mean; r[1] = 1.0f / sqrtf(q * (1.f / 1024.f) + 1e-5f);
  return r;
}

__global__ __launch_bounds__(256) void k0_prep(const u16* __restrict__ x, const u16* __restrict__ wqkv, const u16* __restrict__ wg, const u16* __restrict__ wu,
                                               float* __restrict__ stats, float* __restrict__ cs) {
  __shared__ float red[32][65];
  const int b = blockIdx.x, t = threadIdx.x;
  if (b < 128) {
    const int row = b * 16 + (t >> 4), tile = t & 15;
    const u32x4* p = (const u32x4*)(x + (size_t)row * D_ + tile * 64);
    u32x4 v[8];
#pragma unroll
    for (int i = 0; i < 8; ++i) v[i] = p[i];
    float s = 0.f;
#pragma unroll
    for (int i = 0; i < 8; ++i)
#pragma unroll
      for (int j = 0; j < 4; ++j) s += bflo(v[i][j]) + bfhi(v[i][j]);
    const float m = s * (1.f / 64.f); float q = 0.f;
#pragma unroll
    for (int i = 0; i < 8; ++i)
#pragma unroll
      for (int j = 0; j < 4; ++j) { const float a = bflo(v[i][j]) - m, c = bfhi(v[i][j]) - m; q += a * a + c * c; }
    f32x2 r = {s, q};
    *(f32x2*)(stats + ((size_t)row * 16 + tile) * 2) = r;
  } else {
    const int pn = b - 128;
    const u16* w; int ld, col, outoff;
    if (pn < 48) { w = wqkv; ld = 3072; col = pn * 64; outoff = col; }
    else if (pn < 104) { w = wg; ld = F_; col = (pn - 48) * 64; outoff = 3072 + col; }
    else { w = wu; ld = F_; col = (pn - 104) * 64; outoff = 3072 + F_ + col; }
    const int c8 = t & 7, r0 = t >> 3;
    float a[8] = {0, 0, 0, 0, 0, 0, 0, 0};
#pragma unroll 8
    for (int k = r0; k < D_; k += 32) {
      const u32x4 v = *(const u32x4*)(w + (size_t)k * ld + col + c8 * 8);
#pragma unroll
      for (int j = 0; j < 4; ++j) { a[2 * j] += bflo(v[j]); a[2 * j + 1] += bfhi(v[j]); }
    }
#pragma unroll
    for (int j = 0; j < 8; ++j) red[r0][c8 * 8 + j] = a[j];
    __syncthreads();
    if (t < 64) { float s = 0.f;
#pragma unroll
      for (int r = 0; r < 32; ++r) s += red[r][t];
      cs[outoff + t] = s; }
  }
}

constexpr int K1_STAGE = 40960, K1_STATS = 3 * K1_STAGE, K1_CS = K1_STATS + 1024, K1_LDS = K1_CS + 768;
__global__ __launch_bounds__(512, 2) void k1_qkv(const u16* __restrict__ x, const u16* __restrict__ w, const float* __restrict__ stats, const float* __restrict__ cs,
                                                 u16* __restrict__ pq, float* __restrict__ kvp) {
  extern __shared__ __attribute__((aligned(16))) char smem[];
  LAS char* lds = (LAS char*)smem;
  const int tid = threadIdx.x, wid = __builtin_amdgcn_readfirstlane(tid >> 6), lane = tid & 63;
  const int grp = wid >> 2, j = wid & 3, wrl = (wid >> 1) & 1, wc = wid & 1;
  const int xcd = blockIdx.x & 7, loc = blockIdx.x >> 3;
  const int rt = (xcd & 1) * 8 + (loc & 7), hp = (xcd >> 1) * 4 + (loc >> 3);
  const int rot = ((loc & 7) + 2 * (loc >> 3)) & 7;
  f32x4 sv[4]; float csv;
  { const float* sp = stats + ((size_t)(rt * 128 + ((tid & 255) >> 1)) * 16 + (tid & 1) * 8) * 2;
    GLOAD4(sv[0], sp, 0); GLOAD4(sv[1], sp, 16); GLOAD4(sv[2], sp, 32); GLOAD4(sv[3], sp, 48);
    const int ci = tid < 192 ? tid : 0; const float* cp = cs + (ci >> 6) * 1024 + hp * 64 + (ci & 63);
    asm volatile("global_load_dword %0, %1, off" : "=v"(csv) : "v"(cp) : "memory"); }
  const rsrc_t rs = grp ? mk_rsrc(x, S_ * D_ * 2) : mk_rsrc(w, D_ * 3072 * 2);
  unsigned voff;
  { const int r = lane >> 3, slot = lane & 7;
    if (grp) voff = r * 2048 + ((slot ^ (r & 6)) * 16);
    else { const int f = ((r >> 1) & 1) | ((j & 1) << 1); voff = r * 6144 + ((slot ^ (2 * f)) * 16); } }
  const int i0 = (j & 1) + 4 * (j >> 1);
  auto ktile = [&](int t) { return (t & 8) | ((t + rot) & 7); };
  auto stageW = [&](int t, int st) {
    const unsigned sb = t < 16 ? (unsigned)((ktile(t) * 64 + i0 * 8) * 6144 + hp * 128) : OOB_S;
#pragma unroll
    for (int pnl = 0; pnl < 3; ++pnl)
#pragma unroll
      for (int u = 0; u < 2; ++u) dma16(rs, lds + st * K1_STAGE + 16384 + pnl * 8192 + (i0 + 2 * u) * 1024, voff, sb + pnl * 2048 + u * 16 * 6144);
  };
  auto stageX = [&](int half, int t, int st) {
    const unsigned sb = t < 16 ? (unsigned)((rt * 128 + half * 64 + j * 16) * 2048 + ktile(t) * 128) : OOB_S;
#pragma unroll
    for (int u = 0; u < 2; ++u) dma16(rs, lds + st * K1_STAGE + half * 8192 + (2 * j + u) * 1024, voff, sb + u * 16384);
  };
  if (grp == 0) { stageW(0, 0); stageW(1, 1); WAITV(12); }
  else { stageX(0, 0, 0); stageX(1, 0, 0); stageX(0, 1, 1); stageX(1, 1, 1); stageX(0, 2, 2); WAITV(10); }
  asm volatile("" : "+v"(sv[0]), "+v"(sv[1]), "+v"(sv[2]), "+v"(sv[3]), "+v"(csv));
  { const f32x2 mr = combine_stats(sv);
    if (tid < 256 && !(tid & 1)) *(LAS f32x2*)(lds + K1_STATS + (tid >> 1) * 8) = mr;
    if (tid < 192) *(LAS float*)(lds + K1_CS + tid * 4) = csv; }
  const int m = lane & 15, g = lane >> 4, q = (lane >> 2) & 3, p = lane & 3;
  int xa[2], wa[2];
#pragma unroll
  for (int s = 0; s < 2; ++s) xa[s] = grp * 8192 + (wrl * 32 + m) * 128 + (((4 * s + g) ^ (m & 6)) * 16);
  { const int f = ((q >> 1) & 1) | ((g & 1) << 1);
#pragma unroll
    for (int nb = 0; nb < 2; ++nb) wa[nb] = 16384 + (8 * g + q) * 128 + (((4 * wc + 2 * nb + (p >> 1)) ^ (2 * f)) * 16) + (p & 1) * 8; }
  f32x4 acc[3][2][2];
#pragma unroll
  for (int a = 0; a < 3; ++a)
#pragma unroll
    for (int b = 0; b < 2; ++b)
#pragma unroll
      for (int c = 0; c < 2; ++c) acc[a][b][c] = (f32x4){0.f, 0.f, 0.f, 0.f};
  if (grp == 0) WAITV(6); else WAITV(6);
  WAITL0();
  BAR();
  if (grp == 1) BAR();
  int st = 0, st2 = 2, st0n = 0;
  for (int t = 0; t < 16; ++t) {
    bf16x8 xf[2][2], wf[2][3][2];
    LAS char* sb = lds + st * K1_STAGE;
#pragma unroll
    for (int s = 0; s < 2; ++s) {
#pragma unroll
      for (int mb = 0; mb < 2; ++mb) xf[s][mb] = ld128(sb + xa[s] + mb * 2048);
#pragma unroll
      for (int pnl = 0; pnl < 3; ++pnl)
#pragma unroll
        for (int nb = 0; nb < 2; ++nb) wf[s][pnl][nb] = ldtr(sb + wa[nb] + pnl * 8192 + s * 4096, 512);
    }
    if (grp == 0) stageW(t + 2, st2);
    else { stageX(1, t + 2, st2); stageX(0, t + 3, st); }
    WAITL0();
    BAR();
    __builtin_amdgcn_s_setprio(1);
#pragma unroll
    for (int s = 0; s < 2; ++s)
#pragma unroll
      for (int mb = 0; mb < 2; ++mb)
#pragma unroll
        for (int nb = 0; nb < 2; ++nb) {
          acc[0][mb][nb] = mfma(wf[s][0][nb], xf[s][mb], acc[0][mb][nb]);
          acc[1][mb][nb] = mfma(xf[s][mb], wf[s][1][nb], acc[1][mb][nb]);
          acc[2][mb][nb] = mfma(xf[s][mb], wf[s][2][nb], acc[2][mb][nb]);
        }
    __builtin_amdgcn_s_setprio(0);
    if (grp == 0) WAITV(6); else WAITV(4);
    BAR();
    st2 = st; st = st == 2 ? 0 : st + 1; (void)st0n;
    st2 = st == 0 ? 2 : st - 1;
  }
  if (grp == 0) BAR();
  WAITV(0);
  BAR();
  const int head = 2 * hp + wc, trow = grp * 64 + wrl * 32;
  const LAS f32x2* stt = (const LAS f32x2*)(lds + K1_STATS);
  const LAS float* csl = (const LAS float*)(lds + K1_CS);
#pragma unroll
  for (int mb = 0; mb < 2; ++mb) {
    const f32x2 mr = stt[trow + mb * 16 + m];
    u32x4 o;
#pragma unroll
    for (int nb = 0; nb < 2; ++nb) {
      const f32x4 c4 = *(const LAS f32x4*)(csl + wc * 32 + nb * 16 + 4 * g);
      f32x4 v;
#pragma unroll
      for (int e = 0; e < 4; ++e) v[e] = phi(mr[1] * (acc[0][mb][nb][e] - mr[0] * c4[e]));
      o[2 * nb] = pk2(v[0], v[1]); o[2 * nb + 1] = pk2(v[2], v[3]);
    }
    *(u32x4*)(pq + ((size_t)((rt * 8 + (trow >> 4) + mb) * 32 + head) * 64 + lane) * 8) = o;
  }
  u32x4 pkf[2], vf[2];
#pragma unroll
  for (int mb = 0; mb < 2; ++mb) {
    const f32x4 a0 = *(const LAS f32x4*)(stt + trow + mb * 16 + 4 * g), a1 = *(const LAS f32x4*)(stt + trow + mb * 16 + 4 * g + 2);
    const float mean[4] = {a0[0], a0[2], a1[0], a1[2]}, rstd[4] = {a0[1], a0[3], a1[1], a1[3]};
#pragma unroll
    for (int nb = 0; nb < 2; ++nb) {
      const float ck = csl[64 + wc * 32 + nb * 16 + m], cv = csl[128 + wc * 32 + nb * 16 + m];
      float kk[4], vv[4];
#pragma unroll
      for (int e = 0; e < 4; ++e) { kk[e] = phi(rstd[e] * (acc[1][mb][nb][e] - mean[e] * ck)); vv[e] = rstd[e] * (acc[2][mb][nb][e] - mean[e] * cv); }
      pkf[nb][2 * mb] = pk2(kk[0], kk[1]); pkf[nb][2 * mb + 1] = pk2(kk[2], kk[3]);
      vf[nb][2 * mb] = pk2(vv[0], vv[1]); vf[nb][2 * mb + 1] = pk2(vv[2], vv[3]);
    }
  }
  const u32x4 ones4 = {0x3f803f80u, 0x3f803f80u, 0x3f803f80u, 0x3f803f80u};
  const f32x4 z4 = {0.f, 0.f, 0.f, 0.f};
#pragma unroll
  for (int a = 0; a < 2; ++a) {
#pragma unroll
    for (int b = 0; b < 2; ++b)
      *(LAS f32x4*)(lds + wid * 6144 + (a * 2 + b) * 1024 + lane * 16) = mfma(__builtin_bit_cast(bf16x8, pkf[a]), __builtin_bit_cast(bf16x8, vf[b]), z4);
    *(LAS f32x4*)(lds + wid * 6144 + (4 + a) * 1024 + lane * 16) = mfma(__builtin_bit_cast(bf16x8, pkf[a]), __builtin_bit_cast(bf16x8, ones4), z4);
  }
  WAITL0();
  BAR();
  { const int hw = tid >> 8, blk = (tid >> 6) & 3;
    f32x4 s4 = *(LAS f32x4*)(lds + hw * 6144 + blk * 1024 + lane * 16);
#pragma unroll
    for (int i = 1; i < 4; ++i) s4 += *(LAS f32x4*)(lds + (hw + 2 * i) * 6144 + blk * 1024 + lane * 16);
    float* dst = kvp + (size_t)(rt * 32 + 2 * hp + hw) * 1056;
    *(f32x4*)(dst + blk * 256 + lane * 4) = s4;
    if ((tid & 255) < 8) { const int a = (tid >> 2) & 1, fq = tid & 3;
      f32x4 zz = *(LAS f32x4*)(lds + hw * 6144 + (4 + a) * 1024 + fq * 256);
#pragma unroll
      for (int i = 1; i < 4; ++i) zz += *(LAS f32x4*)(lds + (hw + 2 * i) * 6144 + (4 + a) * 1024 + fq * 256);
      *(f32x4*)(dst + 1024 + a * 16 + fq * 4) = zz; } }
}

__global__ __launch_bounds__(64) void k1b_reduce(const float* __restrict__ kvp, u16* __restrict__ kvt) {
  const int h = blockIdx.x >> 2, blk = blockIdx.x & 3, lane = threadIdx.x;
  const float* src = kvp + (size_t)h * 1056 + blk * 256 + lane * 4;
  f32x4 v[16];
#pragma unroll
  for (int i = 0; i < 16; ++i) v[i] = *(const f32x4*)(src + (size_t)i * 32 * 1056);
  f32x4 zv[16];
  const bool dz = blk == 0 && lane < 8;
  if (dz) {
#pragma unroll
    for (int i = 0; i < 16; ++i) zv[i] = *(const f32x4*)(kvp + (size_t)h * 1056 + 1024 + lane * 4 + (size_t)i * 32 * 1056);
  }
  f32x4 s = v[0];
#pragma unroll
  for (int i = 1; i < 16; ++i) s += v[i];
  const int a = blk >> 1, b = blk & 1, fq = lane >> 4, e = 16 * b + (lane & 15);
  const int i16 = (e >> 3) * 4 + (e & 3), bp = (e >> 2) & 1;
  u32x2 o = {pk2(s[0], s[1]), pk2(s[2], s[3])};
  *(u32x2*)(kvt + ((size_t)(h * 2 + bp) * 64 + fq * 16 + i16) * 8 + 4 * a) = o;
  if (dz) {
    f32x4 z = zv[0];
#pragma unroll
    for (int i = 1; i < 16; ++i) z += zv[i];
    u32x2 oz = {pk2(z[0], z[1]), pk2(z[2], z[3])};
    *(u32x2*)(kvt + 32768 + (h * 4 + (lane & 3)) * 8 + 4 * (lane >> 2)) = oz;
  }
}

constexpr int K24_NS = 4, K24_KV = K24_NS * 16384, K24_LDS = K24_KV + 65536 + 2048;
template <bool ATTN>
__global__ __launch_bounds__(256, 1) void k24_proj(const u16* __restrict__ A, const u16* __restrict__ W, const u16* __restrict__ kvt, const u16* __restrict__ xin,
                                                   u16* __restrict__ xout, float* __restrict__ stats) {
  constexpr int NT = ATTN ? 8 : 28, WIN = ATTN ? 8 : 7, MSTRIDE = NT * 4096;
  extern __shared__ __attribute__((aligned(16))) char smem[];
  LAS char* lds = (LAS char*)smem;
  const int tid = threadIdx.x, wid = __builtin_amdgcn_readfirstlane(tid >> 6), lane = tid & 63;
  const int xcd = blockIdx.x & 7, loc = blockIdx.x >> 3;
  const int rt = (xcd & 3) * 4 + (loc & 3), ct = (xcd >> 2) * 8 + (loc >> 2);
  const int rot = ((loc >> 2) + 2 * (loc & 3)) % WIN;
  const rsrc_t rW = mk_rsrc(W, NT * 128 * D_ * 2);
  unsigned voff;
  { const int r = lane >> 3, slot = lane & 7, f = ((r >> 1) & 1) | ((wid & 1) << 1); voff = r * 2048 + ((slot ^ (2 * f)) * 16); }
  const int i0 = (wid & 1) + 8 * (wid >> 1);
  auto ktile = [&](int t) { const int w0 = (t / WIN) * WIN; int r = t - w0 + rot; if (r >= WIN) r -= WIN; return w0 + r; };
  auto stageW = [&](int t) {
    const unsigned sb = t < NT ? (unsigned)((ktile(t) * 128 + i0 * 8) * 2048 + ct * 128) : OOB_S;
#pragma unroll
    for (int u = 0; u < 4; ++u) dma16(rW, lds + (t & (K24_NS - 1)) * 16384 + (i0 + 2 * u) * 1024, voff, sb + u * 16 * 2048);
  };
  const char* abase = (const char*)A + (size_t)(rt * 8 + 2 * wid) * MSTRIDE + lane * 16;
  u32x4 xf[2][4][2];
#define K24_LOADX(set, ks, t) do { const char* _p = abase + (size_t)ktile((t) < NT ? (t) : NT - 1) * 4096; \
    GLOAD4(xf[set][ks][0], _p, (ks) * 1024); GLOAD4(xf[set][ks][1], _p + MSTRIDE, (ks) * 1024); } while (0)
  if (ATTN) {
    const rsrc_t rK = mk_rsrc(kvt, 65536 + 2048);
#pragma unroll
    for (int u = 0; u < 17; ++u) { const int c = u < 16 ? wid * 16 + u : 64 + (wid & 1); dma16(rK, lds + K24_KV + c * 1024, lane * 16, c * 1024); }
  }
  stageW(0);
#pragma unroll
  for (int ks = 0; ks < 4; ++ks) K24_LOADX(0, ks, 0);
  stageW(1);
#pragma unroll
  for (int ks = 0; ks < 4; ++ks) K24_LOADX(1, ks, 1);
  const int m = lane & 15, g = lane >> 4, q = (lane >> 2) & 3, p = lane & 3;
  int wa[4];
  { const int f = ((q >> 1) & 1) | ((g & 1) << 1);
#pragma unroll
    for (int nb = 0; nb < 4; ++nb) wa[nb] = (8 * g + q) * 128 + (((4 * (nb >> 1) + p) ^ (2 * f)) * 16) + (((nb & 1) ^ (p >> 1)) * 8); }
  f32x4 acc[2][4];
#pragma unroll
  for (int a = 0; a < 2; ++a)
#pragma unroll
    for (int b = 0; b < 4; ++b) acc[a][b] = (f32x4){0.f, 0.f, 0.f, 0.f};
  const f32x4 z4 = {0.f, 0.f, 0.f, 0.f};
  for (int t2 = 0; t2 < NT; t2 += 2) {
#pragma unroll
    for (int set = 0; set < 2; ++set) {
      const int t = t2 + set;
      WAITV(20);
      WAITL0();
      BAR();
      stageW(t + 2);
      LAS char* sb = lds + (t & (K24_NS - 1)) * 16384;
      const int hbase = ktile(t) * 4;
#pragma unroll
      for (int ks = 0; ks < 4; ++ks) {
        bf16x8 wf[4];
#pragma unroll
        for (int nb = 0; nb < 4; ++nb) wf[nb] = ldtr(sb + wa[nb] + ks * 4096, 512);
        bf16x8 kf[2], zf;
        if (ATTN) {
          kf[0] = ld128(lds + K24_KV + (hbase + ks) * 2048 + lane * 16); kf[1] = ld128(lds + K24_KV + (hbase + ks) * 2048 + 1024 + lane * 16);
          zf = ld128(lds + K24_KV + 65536 + (hbase + ks) * 64 + g * 16);
        }
        asm volatile("s_waitcnt vmcnt(22)" : "+v"(xf[set][ks][0]), "+v"(xf[set][ks][1]) :: "memory");
#pragma unroll
        for (int mb = 0; mb < 2; ++mb) {
          bf16x8 bf = __builtin_bit_cast(bf16x8, xf[set][ks][mb]);
          if (ATTN) {
            const f32x4 d0 = mfma(kf[0], bf, z4), d1 = mfma(kf[1], bf, z4), dn = mfma(zf, bf, z4);
            const float r = 1.0f / (dn[0] + 1e-6f);
            u32x4 o = {pk2(d0[0] * r, d0[1] * r), pk2(d0[2] * r, d0[3] * r), pk2(d1[0] * r, d1[1] * r), pk2(d1[2] * r, d1[3] * r)};
            bf = __builtin_bit_cast(bf16x8, o);
          }
#pragma unroll
          for (int nb = 0; nb < 4; ++nb) acc[mb][nb] = mfma(wf[nb], bf, acc[mb][nb]);
        }
        FENCE();
        K24_LOADX(set, ks, t + 2);
      }
    }
  }
  WAITV(0);
#pragma unroll
  for (int s = 0; s < 2; ++s)
#pragma unroll
    for (int ks = 0; ks < 4; ++ks) asm volatile("" : "+v"(xf[s][ks][0]), "+v"(xf[s][ks][1]));
#undef K24_LOADX
  const bool hi = g >= 2;
#pragma unroll
  for (int mb = 0; mb < 2; ++mb) {
    const int row = rt * 128 + wid * 32 + mb * 16 + m;
    const size_t off = (size_t)row * D_ + ct * 64 + 8 * g;
    u32x4 o[2];
#pragma unroll
    for (int pr = 0; pr < 2; ++pr) {
      const u32x4 r4 = *(const u32x4*)(xin + off + pr * 32);
      const f32x4 lo = hi ? acc[mb][2 * pr + 1] : acc[mb][2 * pr], hh = hi ? acc[mb][2 * pr] : acc[mb][2 * pr + 1];
      o[pr][0] = pk2(bflo(r4[0]) + lo[0], bfhi(r4[0]) + lo[1]); o[pr][1] = pk2(bflo(r4[1]) + lo[2], bfhi(r4[1]) + lo[3]);
      o[pr][2] = pk2(bflo(r4[2]) + hh[0], bfhi(r4[2]) + hh[1]); o[pr][3] = pk2(bflo(r4[3]) + hh[2], bfhi(r4[3]) + hh[3]);
      *(u32x4*)(xout + off + pr * 32) = o[pr];
    }
    float s = 0.f;
#pragma unroll
    for (int pr = 0; pr < 2; ++pr)
#pragma unroll
      for (int e = 0; e < 4; ++e) s += bflo(o[pr][e]) + bfhi(o[pr][e]);
    s += __shfl_xor(s, 16); s += __shfl_xor(s, 32);
    const float mean = s * (1.f / 64.f); float qq = 0.f;
#pragma unroll
    for (int pr = 0; pr < 2; ++pr)
#pragma unroll
      for (int e = 0; e < 4; ++e) { const float a = bflo(o[pr][e]) - mean, b = bfhi(o[pr][e]) - mean; qq += a * a + b * b; }
    qq += __shfl_xor(qq, 16); qq += __shfl_xor(qq, 32);
    if (g == 0) { f32x2 r = {s, qq}; *(f32x2*)(stats + ((size_t)row * 16 + ct) * 2) = r; }
  }
}

constexpr int K3_STATS = 131072, K3_CS = K3_STATS + 2048, K3_LDS = K3_CS + 896;
__global__ __launch_bounds__(512, 2) void k3_gateup(const u16* __restrict__ x, const u16* __restrict__ wg, const u16* __restrict__ wu, const float* __restrict__ stats,
                                                    const float* __restrict__ cs, u16* __restrict__ h) {
  extern __shared__ __attribute__((aligned(16))) char smem[];
  LAS char* lds = (LAS char*)smem;
  const int tid = threadIdx.x, wid = __builtin_amdgcn_readfirstlane(tid >> 6), lane = tid & 63;
  const int grp = wid >> 2, j = wid & 3, wrl = (wid >> 1) & 1, wc = wid & 1;
  const int xcd = blockIdx.x & 7, loc = blockIdx.x >> 3;
  const int rt = (xcd & 1) * 4 + (loc & 3), ct = (xcd >> 1) * 8 + (loc >> 2);
  const int rot = ((loc >> 2) + 2 * (loc & 3)) & 7;
  f32x4 sv[4]; float csv;
  { const float* sp = stats + ((size_t)(rt * 256 + (tid >> 1)) * 16 + (tid & 1) * 8) * 2;
    GLOAD4(sv[0], sp, 0); GLOAD4(sv[1], sp, 16); GLOAD4(sv[2], sp, 32); GLOAD4(sv[3], sp, 48);
    const int ci = tid < 224 ? tid : 0; const float* cp = cs + 3072 + (ci >= 112 ? F_ - 112 : 0) + ct * 112 + ci;
    asm volatile("global_load_dword %0, %1, off" : "=v"(csv) : "v"(cp) : "memory"); }
  const int mt = j & 1, hi2 = j >> 1;
  const u16* wp = mt ? wu : wg;
  unsigned voffA, voffB;
  { const int r = lane >> 3, slot = lane & 7; voffA = r * 2048 + ((slot ^ (r & 6)) * 16);
    const int qq = lane >> 4, sl = lane & 15, f = qq | (hi2 << 2), ch = sl ^ (2 * f) ^ mt; voffB = ch < 14 ? (unsigned)(qq * 7168 + ch * 16) : OOB_V; }
  const rsrc_t rX = mk_rsrc(x, S_ * D_ * 2), rW = mk_rsrc(wp, D_ * F_ * 2);
  auto ktile = [&](int t) { return (t & 8) | ((t + rot) & 7); };
  auto stageA = [&](int half, int t) {
    const unsigned sb = t < 16 ? (unsigned)((rt * 256 + half * 128 + j * 32) * 2048 + ktile(t) * 128) : OOB_S;
#pragma unroll
    for (int u = 0; u < 4; ++u) dma16(rX, lds + half * 32768 + (t & 1) * 16384 + (4 * j + u) * 1024, voffA, sb + u * 16384);
  };
  auto stageB = [&](int s, int t) {
    const unsigned sb = t < 16 ? (unsigned)((ktile(t) * 64 + s * 32 + hi2 * 8) * 7168 + ct * 224) : OOB_S;
#pragma unroll
    for (int u = 0; u < 4; ++u) { const int i = (u & 1) + 4 * (u >> 1);
      dma16(rW, lds + 65536 + ((t & 1) * 2 + s) * 16384 + mt * 8192 + (i + 2 * hi2) * 1024, voffB, sb + i * 4 * 7168); }
  };
  if (grp == 0) { stageB(0, 0); stageA(1, 0); stageB(0, 1); }
  else { stageA(0, 0); stageB(1, 0); stageA(0, 1); }
  WAITV(12);
  asm volatile("" : "+v"(sv[0]), "+v"(sv[1]), "+v"(sv[2]), "+v"(sv[3]), "+v"(csv));
  { const f32x2 mr = combine_stats(sv);
    if (!(tid & 1)) *(LAS f32x2*)(lds + K3_STATS + (tid >> 1) * 8) = mr;
    if (tid < 224) *(LAS float*)(lds + K3_CS + tid * 4) = csv; }
  const int m = lane & 15, g = lane >> 4, q = (lane >> 2) & 3, p = lane & 3;
  int xa[2], wa[7];
#pragma unroll
  for (int s = 0; s < 2; ++s) xa[s] = grp * 32768 + (wrl * 64 + m) * 128 + (((4 * s + g) ^ (m & 6)) * 16);
  { const int f2 = 2 * (q | ((g & 1) << 2)), ro = 65536 + (8 * g + q) * 256, c0 = 7 * wc;
#pragma unroll
    for (int mm = 0; mm < 2; ++mm) {
#pragma unroll
      for (int b = 0; b < 2; ++b) wa[3 * mm + b] = ro + mm * 8192 + (((c0 + p) ^ f2 ^ mm) * 16) + ((b ^ (p >> 1)) * 8);
      wa[3 * mm + 2] = ro + mm * 8192 + (((c0 + 4 + (p >> 1)) ^ f2 ^ mm) * 16) + (p & 1) * 8;
    }
    wa[6] = ro + (p >> 1) * 8192 + (((c0 + 6) ^ f2 ^ (p >> 1)) * 16) + (p & 1) * 8; }
  f32x4 acc[4][7];
#pragma unroll
  for (int a = 0; a < 4; ++a)
#pragma unroll
    for (int b = 0; b < 7; ++b) acc[a][b] = (f32x4){0.f, 0.f, 0.f, 0.f};
  WAITV(8);
  WAITL0();
  BAR();
  if (grp == 1) BAR();
  for (int t2 = 0; t2 < 16; t2 += 2) {
#pragma unroll
    for (int buf = 0; buf < 2; ++buf) {
      const int t = t2 + buf;
#pragma unroll
      for (int s = 0; s < 2; ++s) {
        bf16x8 xf[4], wf[7];
#pragma unroll
        for (int mb = 0; mb < 4; ++mb) xf[mb] = ld128(lds + xa[s] + buf * 16384 + mb * 2048);
#pragma unroll
        for (int T = 0; T < 7; ++T) wf[T] = ldtr(lds + wa[T] + (buf * 2 + s) * 16384, 1024);
        if (s == 0) { if (grp == 0) stageA(1, t + 1); else stageB(1, t + 1); }
        else { if (grp == 0) stageB(0, t + 2); else stageA(0, t + 2); }
        WAITV(8);
        WAITL0();
        BAR();
        __builtin_amdgcn_s_setprio(1);
#pragma unroll
        for (int mb = 0; mb < 4; ++mb)
#pragma unroll
          for (int T = 0; T < 7; ++T) acc[mb][T] = mfma(wf[T], xf[mb], acc[mb][T]);
        __builtin_amdgcn_s_setprio(0);
        BAR();
      }
    }
  }
  if (grp == 0) BAR();
  WAITV(0);
  const LAS f32x2* stt = (const LAS f32x2*)(lds + K3_STATS);
  const LAS float* csl = (const LAS float*)(lds + K3_CS);
  f32x4 c4[7];
#pragma unroll
  for (int mm = 0; mm < 2; ++mm) {
#pragma unroll
    for (int b = 0; b < 2; ++b) c4[3 * mm + b] = *(const LAS f32x4*)(csl + mm * 112 + wc * 56 + 8 * g + 4 * (b ^ (g >> 1)));
    c4[3 * mm + 2] = *(const LAS f32x4*)(csl + mm * 112 + wc * 56 + 32 + 4 * g);
  }
  c4[6] = *(const LAS f32x4*)(csl + (g >> 1) * 112 + wc * 56 + 48 + 4 * (g & 1));
  const bool hi = g >= 2;
  const int mblk0 = rt * 16 + grp * 8 + wrl * 4, G0 = 7 * (2 * ct + wc);
  char* hb = (char*)h + (size_t)mblk0 * 114688 + G0 * 256 + m * 16;
  f32x4 zv[4];
#pragma unroll
  for (int mb = 0; mb < 4; ++mb) {
    const f32x2 mr = stt[grp * 128 + wrl * 64 + mb * 16 + m];
    u32x2 hv[3];
#pragma unroll
    for (int b = 0; b < 3; ++b) {
      const f32x4 gg = (acc[mb][b] - mr[0] * c4[b]) * mr[1], uu = (acc[mb][3 + b] - mr[0] * c4[3 + b]) * mr[1], hh = gg * uu;
      hv[b][0] = pk2(hh[0], hh[1]); hv[b][1] = pk2(hh[2], hh[3]);
    }
    zv[mb] = (acc[mb][6] - mr[0] * c4[6]) * mr[1];
    const u32x2 a = hi ? hv[1] : hv[0], b = hi ? hv[0] : hv[1];
    const u32x4 o = {a[0], a[1], b[0], b[1]};
    *(u32x4*)(hb + (size_t)mb * 114688 + g * 256) = o;
    *(u32x2*)(hb + (size_t)mb * 114688 + (4 + (g >> 1)) * 256 + (g & 1) * 8) = hv[2];
  }
#pragma unroll
  for (int mp = 0; mp < 2; ++mp) {
    float hh[4];
#pragma unroll
    for (int e = 0; e < 4; ++e) {
      const auto r = __builtin_amdgcn_permlane32_swap(__float_as_uint(zv[2 * mp][e]), __float_as_uint(zv[2 * mp + 1][e]), false, false);
      hh[e] = __uint_as_float(r[0]) * __uint_as_float(r[1]);
    }
    const u32x2 o = {pk2(hh[0], hh[1]), pk2(hh[2], hh[3])};
    *(u32x2*)(hb + (size_t)(2 * mp + (g >> 1)) * 114688 + 6 * 256 + (g & 1) * 8) = o;
  }
}

extern "C" void kernel_launch(void* const* d_in, const int* in_sizes, int n_in, void* d_out, int out_size, void* d_ws, size_t ws_size, hipStream_t stream) {
  const u16* x0 = (const u16*)d_in[0];
  const u16* wqkv = (const u16*)d_in[1];
  const u16* wo = (const u16*)d_in[2];
  const u16* wg = (const u16*)d_in[4];
  const u16* wu = (const u16*)d_in[5];
  const u16* wd = (const u16*)d_in[6];
  char* ws = (char*)d_ws;
  u16* pq = (u16*)(ws);
  u16* xa = (u16*)(ws + (4 << 20));
  u16* xb = (u16*)(ws + (8 << 20));
  u16* xc = (u16*)(ws + (12 << 20));
  u16* hbuf = (u16*)(ws + (16 << 20));
  float* kvp = (float*)(ws + (32 << 20));
  u16* kvt = (u16*)(ws + (35 << 20));
  float* st = (float*)(ws + (36 << 20));
  float* cs = (float*)(ws + (38 << 20));
  (void)hipFuncSetAttribute((const void*)k1_qkv, hipFuncAttributeMaxDynamicSharedMemorySize, K1_LDS);
  (void)hipFuncSetAttribute((const void*)k3_gateup, hipFuncAttributeMaxDynamicSharedMemorySize, K3_LDS);
  (void)hipFuncSetAttribute((const void*)k24_proj<true>, hipFuncAttributeMaxDynamicSharedMemorySize, K24_LDS);
  (void)hipFuncSetAttribute((const void*)k24_proj<false>, hipFuncAttributeMaxDynamicSharedMemorySize, K24_LDS);
  k0_prep<<<288, 256, 0, stream>>>(x0, wqkv, wg, wu, st, cs);
  const u16* xin[2] = {x0, xb};
  u16* xmid[2] = {xa, xc};
  u16* xo[2] = {xb, (u16*)d_out};
  for (int l = 0; l < 2; ++l) {
    float* s0 = st + (size_t)(2 * l) * 65536; float* s1 = s0 + 65536; float* s2 = st + (size_t)((2 * l + 2) & 3) * 65536;
    k1_qkv<<<256, 512, K1_LDS, stream>>>(xin[l], wqkv, s0, cs, pq, kvp);
    k1b_reduce<<<128, 64, 0, stream>>>(kvp, kvt);
    k24_proj<true><<<256, 256, K24_LDS, stream>>>(pq, wo, kvt, xin[l], xmid[l], s1);
    k3_gateup<<<256, 512, K3_LDS, stream>>>(xmid[l], wg, wu, s1, cs, hbuf);
    k24_proj<false><<<256, 256, K24_LDS, stream>>>(hbuf, wd, kvt, xmid[l], xo[l], s2);
  }
}
```

```cpp
#include <hip/hip_runtime.h>
#include <hip/hip_bf16.h>
#include <stdint.h>

#define LAS __attribute__((address_space(3)))
#define DI __device__ __forceinline__
typedef unsigned short u16;
typedef __bf16 bf16x8 __attribute__((ext_vector_type(8)));
typedef __bf16 bf16x2 __attribute__((ext_vector_type(2)));
typedef short s16x4 __attribute__((ext_vector_type(4)));
typedef short s16x8 __attribute__((ext_vector_type(8)));
typedef float f32x2 __attribute__((ext_vector_type(2)));
typedef float f32x4 __attribute__((ext_vector_type(4)));
typedef unsigned u32x2 __attribute__((ext_vector_type(2)));
typedef unsigned u32x4 __attribute__((ext_vector_type(4)));
typedef u32x4 rsrc_t;

constexpr int S_ = 2048, D_ = 1024, F_ = 3584;
#define ROT_K1 0
#define ROT_K3 0
#define ROT_K2 0
#define ROT_K4 0
#define KTM(x) (x)
constexpr unsigned OOB_V = 0x80000000u, OOB_S = 0x7f000000u;

DI rsrc_t mk_rsrc(const void* p, unsigned bytes) {
  const unsigned long long a = (unsigned long long)p;
  rsrc_t r = {(unsigned)a, (unsigned)(a >> 32) & 0xffffu, bytes, 0x00020000u};
  return r;
}
DI void dma16(rsrc_t r, LAS char* dst, unsigned voff, unsigned soff) {
  asm volatile("s_mov_b32 m0, %0\n\ts_nop 0\n\tbuffer_load_dwordx4 %1, %2, %3 offen lds" :: "s"((unsigned)(size_t)dst), "v"(voff), "s"(r), "s"(soff) : "memory");
}
DI f32x4 mfma(bf16x8 a, bf16x8 b, f32x4 c) {
  return __builtin_amdgcn_mfma_f32_16x16x32_bf16(a, b, c, 0, 0, 0); }
DI bf16x8 ld128(LAS char* p) { return *(LAS bf16x8*)p; }
#define ldtr_real ldtr
DI bf16x8 ldtr_real(LAS char* p, int off2) {
  s16x4 a = __builtin_amdgcn_ds_read_tr16_b64_v4i16((LAS s16x4*)p);
  s16x4 b = __builtin_amdgcn_ds_read_tr16_b64_v4i16((LAS s16x4*)(p + off2));
  return __builtin_bit_cast(bf16x8, __builtin_shufflevector(a, b, 0, 1, 2, 3, 4, 5, 6, 7));
}
DI unsigned pk2(float a, float b) { f32x2 v = {a, b}; return __builtin_bit_cast(unsigned, __builtin_convertvector(v, bf16x2)); }
DI float bflo(unsigned u) { return __uint_as_float(u << 16); }
DI float bfhi(unsigned u) { return __uint_as_float(u & 0xffff0000u); }
DI float phi(float t) { return (t > 0.f ? t + 1.f : __expf(t)) + 1e-6f; }
DI float xsum(float s) {
  const auto a = __builtin_amdgcn_permlane16_swap(__float_as_uint(s), __float_as_uint(s), false, false);
  s = __uint_as_float(a[0]) + __uint_as_float(a[1]);
  const auto b = __builtin_amdgcn_permlane32_swap(__float_as_uint(s), __float_as_uint(s), false, false);
  return __uint_as_float(b[0]) + __uint_as_float(b[1]);
}
#define SGB(mask, n) __builtin_amdgcn_sched_group_barrier(mask, n, 0)
#define FENCE() asm volatile("" ::: "memory")
#define BAR() do { FENCE(); __builtin_amdgcn_s_barrier(); FENCE(); __builtin_amdgcn_sched_barrier(0); } while (0)
#define WAITV(n) asm volatile("s_waitcnt vmcnt(%0)" :: "i"(n) : "memory")
#define WAITL0() do { FENCE(); __builtin_amdgcn_s_waitcnt(0xc07f); FENCE(); } while (0)
#define GLOAD4(dst, ptr, off) asm volatile("global_load_dwordx4 %0, %1, off offset:%2" : "=v"(dst) : "v"(ptr), "i"(off) : "memory")

#define STAMP_DECL
#define STAMP_AT(i)
#define STAMP_END(slot)
#define STAMP_ARG
#define STAMP_PASS(x)

DI f32x2 combine_stats(const f32x4 (&v)[4]) {
  float s = (v[0][0] + v[0][2]) + (v[1][0] + v[1][2]) + (v[2][0] + v[2][2]) + (v[3][0] + v[3][2]);
  s += __shfl_xor(s, 1);
  const float mean = s * (1.f / 1024.f);
  float q = 0.f;
#pragma unroll
  for (int i = 0; i < 4; ++i) {
    const float d0 = v[i][0] * (1.f / 64.f) - mean, d1 = v[i][2] * (1.f / 64.f) - mean;
    q += v[i][1] + v[i][3] + 64.f * (d0 * d0 + d1 * d1);
  }
  q += __shfl_xor(q, 1);
  f32x2 r; r[0] = mean; r[1] = 1.0f / sqrtf(q * (1.f / 1024.f) + 1e-5f);
  return r;
}

__global__ __launch_bounds__(256) void k0_prep(const u16* __restrict__ x, const u16* __restrict__ wqkv, const u16* __restrict__ wg, const u16* __restrict__ wu,
                                               float* __restrict__ stats, float* __restrict__ cs STAMP_ARG) {
  __shared__ float red[32][65];
  STAMP_DECL STAMP_AT(0); STAMP_AT(1); STAMP_AT(2);
  const int b = blockIdx.x, t = threadIdx.x;
  if (b < 128) {
    const int row = b * 16 + (t >> 4), tile = t & 15;
    const u32x4* p = (const u32x4*)(x + (size_t)row * D_ + tile * 64);
    u32x4 v[8];
#pragma unroll
    for (int i = 0; i < 8; ++i) v[i] = p[i];
    float s = 0.f;
#pragma unroll
    for (int i = 0; i < 8; ++i)
#pragma unroll
      for (int j = 0; j < 4; ++j) s += bflo(v[i][j]) + bfhi(v[i][j]);
    const float m = s * (1.f / 64.f); float q = 0.f;
#pragma unroll
    for (int i = 0; i < 8; ++i)
#pragma unroll
      for (int j = 0; j < 4; ++j) { const float a = bflo(v[i][j]) - m, c = bfhi(v[i][j]) - m; q += a * a + c * c; }
    f32x2 r = {s, q};
    *(f32x2*)(stats + ((size_t)row * 16 + tile) * 2) = r;
  } else {
    const int pn = b - 128;
    const u16* w; int ld, col, outoff;
    if (pn < 48) { w = wqkv; ld = 3072; col = pn * 64; outoff = col; }
    else if (pn < 104) { w = wg; ld = F_; col = (pn - 48) * 64; outoff = 3072 + col; }
    else { w = wu; ld = F_; col = (pn - 104) * 64; outoff = 3072 + F_ + col; }
    const int c8 = t & 7, r0 = t >> 3;
    float a[8] = {0, 0, 0, 0, 0, 0, 0, 0};
#pragma unroll 8
    for (int k = r0; k < D_; k += 32) {
      const u32x4 v = *(const u32x4*)(w + (size_t)k * ld + col + c8 * 8);
#pragma unroll
      for (int j = 0; j < 4; ++j) { a[2 * j] += bflo(v[j]); a[2 * j + 1] += bfhi(v[j]); }
    }
#pragma unroll
    for (int j = 0; j < 8; ++j) red[r0][c8 * 8 + j] = a[j];
    __syncthreads();
    if (t < 64) { float s = 0.f;
#pragma unroll
      for (int r = 0; r < 32; ++r) s += red[r][t];
      cs[outoff + t] = s; }
  }
  STAMP_END(slot);
}

constexpr int K1_STAGE = 40960, K1_STATS = 3 * K1_STAGE, K1_CS = K1_STATS + 1024, K1_RED = K1_CS + 1024, K1_LDS = K1_RED + 12288;
__global__ __launch_bounds__(512, 2) void k1_qkv(const u16* __restrict__ x, const u16* __restrict__ w, const float* __restrict__ stats, const float* __restrict__ cs,
                                                 u16* __restrict__ pq, float* __restrict__ kvp STAMP_ARG) {
  extern __shared__ __attribute__((aligned(16))) char smem[];
  STAMP_DECL STAMP_AT(0);
  LAS char* lds = (LAS char*)smem;
  const int tid = threadIdx.x, wid = __builtin_amdgcn_readfirstlane(tid >> 6), lane = tid & 63;
  const int xcd = blockIdx.x & 7, loc = blockIdx.x >> 3;
  const int rt = (xcd & 1) * 8 + (loc & 7), hp = (xcd >> 1) * 4 + (loc >> 3);
  if (wid >= 4) {
    const int j = wid & 3;
    const int rot = ROT_K1 ? ((loc & 7) + 2 * (loc >> 3)) & 7 : 0;
    const rsrc_t rX = mk_rsrc(x, S_ * D_ * 2), rW = mk_rsrc(w, D_ * 3072 * 2);
    const int r = lane >> 3, slot = lane & 7, f = ((r >> 1) & 1) | ((j & 1) << 1);
    const unsigned voffX = r * 2048 + ((slot ^ (r & 6)) * 16), voffW = r * 6144 + ((slot ^ (2 * f)) * 16);
    auto stage = [&](int t, int st) {
      const int kt = KTM((t & 8) | ((t + rot) & 7));
      const unsigned sx = t < 16 ? (unsigned)((rt * 128 + j * 32) * 2048 + kt * 128) : OOB_S, sw = t < 16 ? (unsigned)((kt * 64 + j * 8) * 6144 + hp * 128) : OOB_S;
      LAS char* b = lds + st * K1_STAGE;
#pragma unroll
      for (int pnl = 0; pnl < 3; ++pnl) dma16(rW, b + 16384 + pnl * 8192 + j * 1024, voffW, sw + pnl * 2048);
#pragma unroll
      for (int u = 0; u < 4; ++u) dma16(rX, b + (4 * j + u) * 1024, voffX, sx + u * 16384);
#pragma unroll
      for (int pnl = 0; pnl < 3; ++pnl) dma16(rW, b + 16384 + pnl * 8192 + (4 + j) * 1024, voffW, sw + pnl * 2048 + 32 * 6144);
    };
    stage(0, 0); stage(1, 1);
    int st = 2;
    for (int t = 0; t < 16; ++t) {
      WAITV(10);
      BAR();
      stage(t + 2, st);
      st = st == 2 ? 0 : st + 1;
    }
    BAR();
    return;
  }
  const int wr = wid >> 1, wc = wid & 1;
  { f32x4 sv[4];
    const float* sp = stats + ((size_t)(rt * 128 + (tid >> 1)) * 16 + (tid & 1) * 8) * 2;
#pragma unroll
    for (int i = 0; i < 4; ++i) sv[i] = *(const f32x4*)(sp + 4 * i);
    const int ci = tid < 192 ? tid : 0; const float csv = cs[(ci >> 6) * 1024 + hp * 64 + (ci & 63)];
    const f32x2 mr = combine_stats(sv);
    if (!(tid & 1)) *(LAS f32x2*)(lds + K1_STATS + (tid >> 1) * 8) = mr;
    if (tid < 192) *(LAS float*)(lds + K1_CS + tid * 4) = csv; }
  const int m = lane & 15, g = lane >> 4, q = (lane >> 2) & 3, p = lane & 3;
  int xa[2], wa[2];
#pragma unroll
  for (int s = 0; s < 2; ++s) xa[s] = (wr * 64 + m) * 128 + (((4 * s + g) ^ (m & 6)) * 16);
  { const int f = ((q >> 1) & 1) | ((g & 1) << 1);
#pragma unroll
    for (int nb = 0; nb < 2; ++nb) wa[nb] = 16384 + (8 * g + q) * 128 + (((4 * wc + 2 * nb + (p >> 1)) ^ (2 * f)) * 16) + (p & 1) * 8; }
  f32x4 acc[3][4][2];
#pragma unroll
  for (int a = 0; a < 3; ++a)
#pragma unroll
    for (int b = 0; b < 4; ++b)
#pragma unroll
      for (int c = 0; c < 2; ++c) acc[a][b][c] = (f32x4){0.f, 0.f, 0.f, 0.f};
  bf16x8 xf[2][4], wf[2][3][2];
  auto fetch = [&](int s, int st) {
    LAS char* sb = lds + st * K1_STAGE;
#pragma unroll
    for (int mb = 0; mb < 4; ++mb) xf[s][mb] = ld128(sb + xa[s] + mb * 2048);
#pragma unroll
    for (int nb = 0; nb < 2; ++nb)
#pragma unroll
      for (int pnl = 0; pnl < 3; ++pnl) wf[s][pnl][nb] = ldtr(sb + wa[nb] + pnl * 8192 + s * 4096, 512);
  };
  auto mma = [&](int s) {
#pragma unroll
    for (int nb = 0; nb < 2; ++nb)
#pragma unroll
      for (int mb = 0; mb < 4; ++mb) {
        acc[0][mb][nb] = mfma(wf[s][0][nb], xf[s][mb], acc[0][mb][nb]);
        acc[1][mb][nb] = mfma(xf[s][mb], wf[s][1][nb], acc[1][mb][nb]);
        acc[2][mb][nb] = mfma(xf[s][mb], wf[s][2][nb], acc[2][mb][nb]);
      }
  };
#define K1_MIX() do { _Pragma("unroll") for (int i_ = 0; i_ < 16; ++i_) { SGB(0x8, 1); SGB(0x100, 1); } SGB(0x8, 8); __builtin_amdgcn_sched_barrier(0); } while (0)
  WAITL0();
  BAR();
  STAMP_AT(1);
  fetch(0, 0);
  __builtin_amdgcn_sched_barrier(0);
  int st = 0;
  for (int t = 0; t < 15; ++t) {
    fetch(1, st); mma(0); K1_MIX();
    st = st == 2 ? 0 : st + 1;
    WAITL0();
    BAR();
    fetch(0, st); mma(1); K1_MIX();
  }
  fetch(1, st); mma(0); K1_MIX();
  mma(1);
  STAMP_AT(2);
  const int head = 2 * hp + wc, trow = wr * 64;
  const LAS f32x2* stt = (const LAS f32x2*)(lds + K1_STATS);
  const LAS float* csl = (const LAS float*)(lds + K1_CS);
  const u32x4 ones4 = {0x3f803f80u, 0x3f803f80u, 0x3f803f80u, 0x3f803f80u};
  f32x4 kvr[6];
#pragma unroll
  for (int i = 0; i < 6; ++i) kvr[i] = (f32x4){0.f, 0.f, 0.f, 0.f};
  float ck[2], cv[2];
#pragma unroll
  for (int nb = 0; nb < 2; ++nb) { ck[nb] = csl[64 + wc * 32 + nb * 16 + m]; cv[nb] = csl[128 + wc * 32 + nb * 16 + m]; }
#pragma unroll
  for (int mp = 0; mp < 2; ++mp) {
    u32x4 pkf[2], vf[2];
#pragma unroll
    for (int hb = 0; hb < 2; ++hb) {
      const int mb = 2 * mp + hb;
      const f32x4 a0 = *(const LAS f32x4*)(stt + trow + mb * 16 + 4 * g), a1 = *(const LAS f32x4*)(stt + trow + mb * 16 + 4 * g + 2);
      const float mean[4] = {a0[0], a0[2], a1[0], a1[2]}, rstd[4] = {a0[1], a0[3], a1[1], a1[3]};
#pragma unroll
      for (int nb = 0; nb < 2; ++nb) {
        float kk[4], vv[4];
#pragma unroll
        for (int e = 0; e < 4; ++e) { kk[e] = phi(rstd[e] * (acc[1][mb][nb][e] - mean[e] * ck[nb])); vv[e] = rstd[e] * (acc[2][mb][nb][e] - mean[e] * cv[nb]); }
        pkf[nb][2 * hb] = pk2(kk[0], kk[1]); pkf[nb][2 * hb + 1] = pk2(kk[2], kk[3]);
        vf[nb][2 * hb] = pk2(vv[0], vv[1]); vf[nb][2 * hb + 1] = pk2(vv[2], vv[3]);
      }
    }
#pragma unroll
    for (int a = 0; a < 2; ++a) {
#pragma unroll
      for (int b = 0; b < 2; ++b) kvr[a * 3 + b] = mfma(__builtin_bit_cast(bf16x8, pkf[a]), __builtin_bit_cast(bf16x8, vf[b]), kvr[a * 3 + b]);
      kvr[a * 3 + 2] = mfma(__builtin_bit_cast(bf16x8, pkf[a]), __builtin_bit_cast(bf16x8, ones4), kvr[a * 3 + 2]);
    }
  }
  f32x4 own[3];
#pragma unroll
  for (int i = 0; i < 3; ++i) {
    f32x4 snd;
#pragma unroll
    for (int e = 0; e < 4; ++e) { snd[e] = wr ? kvr[i][e] : kvr[3 + i][e]; own[i][e] = wr ? kvr[3 + i][e] : kvr[i][e]; }
    *(LAS f32x4*)(lds + K1_RED + wid * 3072 + i * 1024 + lane * 16) = snd;
  }
  f32x4 c4[2];
#pragma unroll
  for (int nb = 0; nb < 2; ++nb) c4[nb] = *(const LAS f32x4*)(csl + wc * 32 + nb * 16 + 4 * g);
#pragma unroll
  for (int mb = 0; mb < 4; ++mb) {
    const f32x2 mr = stt[trow + mb * 16 + m];
    u32x4 o;
#pragma unroll
    for (int nb = 0; nb < 2; ++nb) {
      f32x4 v;
#pragma unroll
      for (int e = 0; e < 4; ++e) v[e] = phi(mr[1] * (acc[0][mb][nb][e] - mr[0] * c4[nb][e]));
      o[2 * nb] = pk2(v[0], v[1]); o[2 * nb + 1] = pk2(v[2], v[3]);
    }
    *(u32x4*)(pq + ((size_t)((rt * 8 + wr * 4 + mb) * 32 + head) * 64 + lane) * 8) = o;
  }
  WAITL0();
  BAR();
  float* dst = kvp + (size_t)(rt * 32 + head) * 1056;
#pragma unroll
  for (int i = 0; i < 3; ++i) own[i] += *(LAS f32x4*)(lds + K1_RED + (wid ^ 2) * 3072 + i * 1024 + lane * 16);
  *(f32x4*)(dst + (wr * 2) * 256 + lane * 4) = own[0];
  *(f32x4*)(dst + (wr * 2 + 1) * 256 + lane * 4) = own[1];
  if (m == 0) *(f32x4*)(dst + 1024 + wr * 16 + g * 4) = own[2];
  STAMP_END(slot);
}

__global__ __launch_bounds__(256) void k1b_o(const float* __restrict__ kvp, u16* __restrict__ pq STAMP_ARG) {
  __shared__ __attribute__((aligned(16))) char sm[2048 + 64];
  STAMP_DECL STAMP_AT(0); STAMP_AT(1); STAMP_AT(2);
  LAS char* lds = (LAS char*)sm;
  const int tid = threadIdx.x, lane = tid & 63, wid = tid >> 6;
  const int h = (blockIdx.x & 7) * 4 + ((blockIdx.x >> 3) & 3), r = blockIdx.x >> 5;
  const float* src = kvp + (size_t)h * 1056 + tid * 4;
  f32x4 v[16], zv[16];
#pragma unroll
  for (int i = 0; i < 16; ++i) v[i] = *(const f32x4*)(src + (size_t)i * 32 * 1056);
  if (tid < 8) {
#pragma unroll
    for (int i = 0; i < 16; ++i) zv[i] = *(const f32x4*)(src + 1024 + (size_t)i * 32 * 1056);
  }
  u16* pp = pq + ((size_t)((r * 16 + wid * 4) * 32 + h) * 64 + lane) * 8;
  bf16x8 xf[4];
#pragma unroll
  for (int mb = 0; mb < 4; ++mb) xf[mb] = *(const bf16x8*)(pp + (size_t)mb * 32 * 512);
  f32x4 s = v[0];
#pragma unroll
  for (int i = 1; i < 16; ++i) s += v[i];
  { const int a = wid >> 1, b = wid & 1, fq = lane >> 4, e = 16 * b + (lane & 15);
    const int i16 = (e >> 3) * 4 + (e & 3), bp = (e >> 2) & 1;
    const u32x2 o = {pk2(s[0], s[1]), pk2(s[2], s[3])};
    *(LAS u32x2*)(lds + bp * 1024 + (fq * 16 + i16) * 16 + 8 * a) = o; }
  if (tid < 8) {
    f32x4 z = zv[0];
#pragma unroll
    for (int i = 1; i < 16; ++i) z += zv[i];
    const u32x2 oz = {pk2(z[0], z[1]), pk2(z[2], z[3])};
    *(LAS u32x2*)(lds + 2048 + (tid & 3) * 16 + 8 * (tid >> 2)) = oz;
  }
  __syncthreads();
  const bf16x8 k0 = *(LAS bf16x8*)(lds + lane * 16), k1 = *(LAS bf16x8*)(lds + 1024 + lane * 16), zf = *(LAS bf16x8*)(lds + 2048 + (lane >> 4) * 16);
  const f32x4 z4 = {0.f, 0.f, 0.f, 0.f};
#pragma unroll
  for (int mb = 0; mb < 4; ++mb) {
    const f32x4 dn = mfma(zf, xf[mb], z4), d0 = mfma(k0, xf[mb], z4), d1 = mfma(k1, xf[mb], z4);
    const float rr = __builtin_amdgcn_rcpf(dn[0] + 1e-6f);
    const u32x4 o = {pk2(d0[0] * rr, d0[1] * rr), pk2(d0[2] * rr, d0[3] * rr), pk2(d1[0] * rr, d1[1] * rr), pk2(d1[2] * rr, d1[3] * rr)};
    *(u32x4*)(pp + (size_t)mb * 32 * 512) = o;
  }
  STAMP_END(slot);
}

template <int N, int NI> DI void drain_bar() { WAITV(N * NI); BAR(); if constexpr (N > 0) drain_bar<N - 1, NI>(); }
DI void res_store(const f32x4 (&a)[4], const u32x4 (&res)[2], u16* xo, float* st, int g) {
  const bool hi = g >= 2;
  u32x4 o[2];
#pragma unroll
  for (int pr = 0; pr < 2; ++pr) {
    const u32x4 r4 = res[pr];
    f32x4 lo, hh;
#pragma unroll
    for (int e = 0; e < 4; ++e) { lo[e] = hi ? a[2 * pr + 1][e] : a[2 * pr][e]; hh[e] = hi ? a[2 * pr][e] : a[2 * pr + 1][e]; }
    o[pr][0] = pk2(bflo(r4[0]) + lo[0], bfhi(r4[0]) + lo[1]); o[pr][1] = pk2(bflo(r4[1]) + lo[2], bfhi(r4[1]) + lo[3]);
    o[pr][2] = pk2(bflo(r4[2]) + hh[0], bfhi(r4[2]) + hh[1]); o[pr][3] = pk2(bflo(r4[3]) + hh[2], bfhi(r4[3]) + hh[3]);
    *(u32x4*)(xo + pr * 32) = o[pr];
  }
  float s = 0.f;
#pragma unroll
  for (int pr = 0; pr < 2; ++pr)
#pragma unroll
    for (int e = 0; e < 4; ++e) s += bflo(o[pr][e]) + bfhi(o[pr][e]);
  s = xsum(s);
  const float mean = s * (1.f / 64.f); float qq = 0.f;
#pragma unroll
  for (int pr = 0; pr < 2; ++pr)
#pragma unroll
    for (int e = 0; e < 4; ++e) { const float x0 = bflo(o[pr][e]) - mean, x1 = bfhi(o[pr][e]) - mean; qq += x0 * x0 + x1 * x1; }
  qq = xsum(qq);
  if (g == 0) { f32x2 r = {s, qq}; *(f32x2*)st = r; }
}
#define SB0() __builtin_amdgcn_sched_barrier(0)
#define PIN(x) asm volatile("" : "+v"(x))

constexpr int K2_NT = 16, K2_STAGE = 24576, K2_NS = 6, K2_LDS = K2_NS * K2_STAGE;
__global__ __launch_bounds__(512, 2) void k2_proj(const u16* __restrict__ A, const u16* __restrict__ W, const u16* __restrict__ xin,
                                                  u16* __restrict__ xout, float* __restrict__ stats STAMP_ARG) {
  extern __shared__ __attribute__((aligned(16))) char smem[];
  STAMP_DECL STAMP_AT(0);
  LAS char* lds = (LAS char*)smem;
  const int tid = threadIdx.x, wid = __builtin_amdgcn_readfirstlane(tid >> 6), lane = tid & 63;
  const int xcd = blockIdx.x & 7, loc = blockIdx.x >> 3;
  const int rt = (xcd & 3) * 4 + (loc & 3), ct = (xcd >> 2) * 8 + (loc >> 2);
  if (wid >= 4) {
    const int j = wid & 3, rot = ROT_K2 ? ((loc >> 2) + 2 * (loc & 3)) & 7 : 0;
    const rsrc_t rA = mk_rsrc(A, S_ * D_ * 2), rW = mk_rsrc(W, D_ * D_ * 2);
    const int r = lane >> 3, slot = lane & 7, f = ((r >> 1) & 1) | ((j & 1) << 1);
    const unsigned voffW = r * 2048 + ((slot ^ (2 * f)) * 16), voffL = lane * 16;
    auto stage = [&](int t, int sl) {
      const int kt = KTM((t & ~7) | ((t + rot) & 7));
      const unsigned sa = (unsigned)((rt * 8 + 2 * j) * 32768 + kt * 2048), sw = (unsigned)((kt * 64 + j * 8) * 2048 + ct * 128);
      LAS char* b = lds + sl * K2_STAGE;
#pragma unroll
      for (int u = 0; u < 4; ++u) dma16(rA, b + (4 * j + u) * 1024, voffL, sa + (u >> 1) * 32768 + (u & 1) * 1024);
#pragma unroll
      for (int u = 0; u < 2; ++u) dma16(rW, b + 16384 + (j + 4 * u) * 1024, voffW, sw + u * 32 * 2048);
    };
#pragma unroll
    for (int t = 0; t < K2_NS - 1; ++t) stage(t, t);
    int sl = K2_NS - 1;
    for (int t = 0; t < K2_NT - K2_NS + 1; ++t) {
      WAITV((K2_NS - 2) * 6);
      BAR();
      stage(t + K2_NS - 1, sl);
      sl = sl == K2_NS - 1 ? 0 : sl + 1;
    }
    drain_bar<K2_NS - 2, 6>();
    return;
  }
  const int m = lane & 15, g = lane >> 4, q = (lane >> 2) & 3, p = lane & 3;
  const int row0 = rt * 128 + wid * 32 + m;
  const size_t off0 = (size_t)row0 * D_ + ct * 64 + 8 * g;
  u32x4 res[2][2];
#pragma unroll
  for (int mb = 0; mb < 2; ++mb) { res[mb][0] = *(const u32x4*)(xin + off0 + mb * 16 * D_); res[mb][1] = *(const u32x4*)(xin + off0 + mb * 16 * D_ + 32); }
  int wa[4];
  { const int f = ((q >> 1) & 1) | ((g & 1) << 1);
#pragma unroll
    for (int nb = 0; nb < 4; ++nb) wa[nb] = 16384 + (8 * g + q) * 128 + (((4 * (nb >> 1) + p) ^ (2 * f)) * 16) + (((nb & 1) ^ (p >> 1)) * 8); }
  const int xa = wid * 4096 + lane * 16;
  f32x4 acc[2][4];
#pragma unroll
  for (int a = 0; a < 2; ++a)
#pragma unroll
    for (int b = 0; b < 4; ++b) acc[a][b] = (f32x4){0.f, 0.f, 0.f, 0.f};
  bf16x8 xf[2][2][2], wf[2][2][4];
  auto fetch = [&](int set, int sl) {
    LAS char* b = lds + sl * K2_STAGE;
#pragma unroll
    for (int s = 0; s < 2; ++s) {
      xf[set][s][0] = ld128(b + xa + s * 1024); xf[set][s][1] = ld128(b + xa + s * 1024 + 2048);
#pragma unroll
      for (int nb = 0; nb < 4; ++nb) wf[set][s][nb] = ldtr(b + wa[nb] + s * 4096, 512);
    }
  };
  auto mma = [&](int set) {
#pragma unroll
    for (int s = 0; s < 2; ++s)
#pragma unroll
      for (int nb = 0; nb < 4; ++nb)
#pragma unroll
        for (int mb = 0; mb < 2; ++mb) acc[mb][nb] = mfma(wf[set][s][nb], xf[set][s][mb], acc[mb][nb]);
  };
#define K2_MIX() do { _Pragma("unroll") for (int i_ = 0; i_ < 10; ++i_) { SGB(0x8, 1); SGB(0x100, 2); } SGB(0x8, 6); SB0(); } while (0)
  BAR();
  STAMP_AT(1);
  fetch(0, 0);
  SB0();
  int sl = 1;
  for (int t = 1; t < K2_NT - 1; t += 2) {
    WAITL0(); BAR();
    fetch(1, sl); mma(0); K2_MIX();
    sl = sl == K2_NS - 1 ? 0 : sl + 1;
    WAITL0(); BAR();
    fetch(0, sl); mma(1); K2_MIX();
    sl = sl == K2_NS - 1 ? 0 : sl + 1;
  }
  WAITL0(); BAR();
  fetch(1, sl); mma(0); K2_MIX();
  mma(1);
  STAMP_AT(2);
#pragma unroll
  for (int mb = 0; mb < 2; ++mb) res_store(acc[mb], res[mb], xout + off0 + mb * 16 * D_, stats + ((size_t)(row0 + mb * 16) * 16 + ct) * 2, g);
  STAMP_END(slot);
}

constexpr int K4_NT = 28, K4_STAGE = 32768, K4_NS = 4, K4_LDS = K4_NS * K4_STAGE;
__global__ __launch_bounds__(512, 2) void k4_down(const u16* __restrict__ A, const u16* __restrict__ W, const u16* __restrict__ xin,
                                                  u16* __restrict__ xout, float* __restrict__ stats, float* xch, unsigned* flags STAMP_ARG) {
  extern __shared__ __attribute__((aligned(16))) char smem[];
  STAMP_DECL STAMP_AT(0);
  LAS char* lds = (LAS char*)smem;
  const int tid = threadIdx.x, wid = __builtin_amdgcn_readfirstlane(tid >> 6), lane = tid & 63;
  const int xcd = blockIdx.x & 7, loc = blockIdx.x >> 3;
  const int kh = xcd & 1, rt = (xcd >> 1) * 4 + (loc & 3), ct = loc >> 2;
  if (wid >= 4) {
    const int j = wid & 3, rot = ROT_K4 ? (ct + 2 * (loc & 3)) % 7 : 0;
    const rsrc_t rA = mk_rsrc(A, S_ * F_ * 2), rW = mk_rsrc(W, F_ * D_ * 2);
    const int r = lane >> 3, slot = lane & 7, f = ((r >> 1) & 1) | ((j & 1) << 1);
    const unsigned voffW = r * 2048 + ((slot ^ (2 * f)) * 16), voffL = lane * 16;
    int w0 = kh * K4_NT, wr_ = rot;
    auto stage = [&](int sl) {
      const int kt = KTM(w0 + wr_);
      const unsigned sa = (unsigned)((rt * 8 + 2 * j) * 114688 + kt * 2048), sw = (unsigned)((kt * 64 + j * 8) * 2048 + ct * 256);
      LAS char* b = lds + sl * K4_STAGE;
#pragma unroll
      for (int u = 0; u < 4; ++u) {
        dma16(rA, b + (4 * j + u) * 1024, voffL, sa + (u >> 1) * 114688 + (u & 1) * 1024);
        dma16(rW, b + 16384 + (u >> 1) * 8192 + (j + 4 * (u & 1)) * 1024, voffW, sw + (u >> 1) * 128 + (u & 1) * 32 * 2048);
      }
      wr_ = wr_ == 6 ? 0 : wr_ + 1; if (wr_ == rot) w0 += 7;
    };
#pragma unroll
    for (int t = 0; t < K4_NS - 1; ++t) stage(t);
    int sl = K4_NS - 1;
    for (int t = 0; t < K4_NT - K4_NS + 1; ++t) {
      WAITV((K4_NS - 2) * 8);
      BAR();
      stage(sl);
      sl = (sl + 1) & (K4_NS - 1);
    }
    drain_bar<K4_NS - 2, 8>();
    return;
  }
  const int wr = wid >> 1, wc = wid & 1;
  const int m = lane & 15, g = lane >> 4, q = (lane >> 2) & 3, p = lane & 3;
  const int row0 = rt * 128 + wr * 64 + kh * 32 + m;
  const size_t off0 = (size_t)row0 * D_ + ct * 128 + wc * 64 + 8 * g;
  u32x4 res[2][2];
#pragma unroll
  for (int mb = 0; mb < 2; ++mb) { res[mb][0] = *(const u32x4*)(xin + off0 + mb * 16 * D_); res[mb][1] = *(const u32x4*)(xin + off0 + mb * 16 * D_ + 32); }
  int wa[4];
  { const int f = ((q >> 1) & 1) | ((g & 1) << 1);
#pragma unroll
    for (int nb = 0; nb < 4; ++nb) wa[nb] = 16384 + wc * 8192 + (8 * g + q) * 128 + (((4 * (nb >> 1) + p) ^ (2 * f)) * 16) + (((nb & 1) ^ (p >> 1)) * 8); }
  const int xa = wr * 8192 + lane * 16, xo0 = kh * 4096, xo1 = 4096 - xo0;
  f32x4 acc[4][4];
#pragma unroll
  for (int a = 0; a < 4; ++a)
#pragma unroll
    for (int b = 0; b < 4; ++b) acc[a][b] = (f32x4){0.f, 0.f, 0.f, 0.f};
  bf16x8 xf[2][4], wf[2][4];
  auto fetch = [&](int s, int sl) {
    LAS char* b = lds + sl * K4_STAGE;
    xf[s][0] = ld128(b + xa + xo0 + s * 1024); xf[s][1] = ld128(b + xa + xo0 + s * 1024 + 2048);
    xf[s][2] = ld128(b + xa + xo1 + s * 1024); xf[s][3] = ld128(b + xa + xo1 + s * 1024 + 2048);
#pragma unroll
    for (int nb = 0; nb < 4; ++nb) wf[s][nb] = ldtr(b + wa[nb] + s * 4096, 512);
  };
  auto mma = [&](int s) {
#pragma unroll
    for (int nb = 0; nb < 4; ++nb)
#pragma unroll
      for (int mb = 0; mb < 4; ++mb) acc[mb][nb] = mfma(wf[s][nb], xf[s][mb], acc[mb][nb]);
  };
#define K4_MIX() do { _Pragma("unroll") for (int i_ = 0; i_ < 12; ++i_) { SGB(0x8, 1); SGB(0x100, 1); } SGB(0x8, 4); SB0(); } while (0)
  BAR();
  STAMP_AT(1);
  fetch(0, 0);
  SB0();
  int sl = 0;
  for (int t = 0; t < K4_NT - 1; ++t) {
    fetch(1, sl); mma(0); K4_MIX();
    sl = (sl + 1) & (K4_NS - 1);
    WAITL0(); BAR();
    fetch(0, sl); mma(1); K4_MIX();
  }
  fetch(1, sl); mma(0); K4_MIX();
  mma(1);
  STAMP_AT(2);
  const int pair = rt * 8 + ct;
  const __amdgpu_buffer_rsrc_t rx = __builtin_amdgcn_make_buffer_rsrc((void*)(xch + (size_t)pair * 16384), 0, 65536, 0x00020000);
#pragma unroll
  for (int i = 0; i < 8; ++i)
    __builtin_amdgcn_raw_buffer_store_b128(__builtin_bit_cast(u32x4, acc[2 + (i >> 2)][i & 3]), rx, lane * 16, ((kh ^ 1) * 4 + wid) * 8192 + i * 1024, 16);
  WAITV(0);
  BAR();
  if (wid == 0) {
    if (lane == 0) __hip_atomic_store(flags + (pair * 2 + kh) * 64, 1u, __ATOMIC_RELAXED, __HIP_MEMORY_SCOPE_AGENT);
    unsigned spins = 0;
    while (__builtin_amdgcn_readfirstlane(__hip_atomic_load(flags + (pair * 2 + (kh ^ 1)) * 64, __ATOMIC_RELAXED, __HIP_MEMORY_SCOPE_AGENT)) != 1u && ++spins < (1u << 20))
      __builtin_amdgcn_s_sleep(1);
  }
  BAR();
  u32x4 in[8];
#pragma unroll
  for (int i = 0; i < 8; ++i) in[i] = __builtin_amdgcn_raw_buffer_load_b128(rx, lane * 16, (kh * 4 + wid) * 8192 + i * 1024, 16);
#pragma unroll
  for (int i = 0; i < 8; ++i) acc[i >> 2][i & 3] += __builtin_bit_cast(f32x4, in[i]);
#pragma unroll
  for (int mb = 0; mb < 2; ++mb) res_store(acc[mb], res[mb], xout + off0 + mb * 16 * D_, stats + ((size_t)(row0 + mb * 16) * 16 + ct * 2 + wc) * 2, g);
  STAMP_END(slot);
}

constexpr int K3_STATS = 131072, K3_CS = K3_STATS + 2048, K3_LDS = K3_CS + 896;
__global__ __launch_bounds__(768, 3) void k3_gateup(const u16* __restrict__ x, const u16* __restrict__ wg, const u16* __restrict__ wu, const float* __restrict__ stats,
                                                    const float* __restrict__ cs, u16* __restrict__ h, unsigned* __restrict__ flags STAMP_ARG) {
  extern __shared__ __attribute__((aligned(16))) char smem[];
  STAMP_DECL STAMP_AT(0);
  LAS char* lds = (LAS char*)smem;
  const int tid = threadIdx.x, wid = __builtin_amdgcn_readfirstlane(tid >> 6), lane = tid & 63;
  const int xcd = blockIdx.x & 7, loc = blockIdx.x >> 3;
  const int rt = (xcd & 1) * 4 + (loc & 3), ct = (xcd >> 1) * 8 + (loc >> 2);
  if (wid >= 8) {
    const int j = wid & 3;
    const int rot = ROT_K3 ? ((loc >> 2) + 2 * (loc & 3)) & 7 : 0;
    const int mt = j & 1, hi2 = j >> 1;
    unsigned voffA, voffB;
    { const int r = lane >> 3, slot = lane & 7; voffA = r * 2048 + ((slot ^ (r & 6)) * 16);
      const int qq = lane >> 4, sl = lane & 15, f = qq | (hi2 << 2), ch = sl ^ (2 * f) ^ mt; voffB = ch < 14 ? (unsigned)(qq * 7168 + ch * 16) : OOB_V; }
    const rsrc_t rX = mk_rsrc(x, S_ * D_ * 2), rW = mk_rsrc(mt ? wu : wg, D_ * F_ * 2);
    auto ktile = [&](int t) { return KTM((t & 8) | ((t + rot) & 7)); };
    auto stageA = [&](int half, int t) {
      const unsigned sb = t < 16 ? (unsigned)((rt * 256 + half * 128 + j * 32) * 2048 + ktile(t) * 128) : OOB_S;
#pragma unroll
      for (int u = 0; u < 4; ++u) dma16(rX, lds + half * 32768 + (t & 1) * 16384 + (4 * j + u) * 1024, voffA, sb + u * 16384);
    };
    auto stageB = [&](int s, int t) {
      const unsigned sb = t < 16 ? (unsigned)((ktile(t) * 64 + s * 32 + hi2 * 8) * 7168 + ct * 224) : OOB_S;
#pragma unroll
      for (int u = 0; u < 4; ++u) { const int i = (u & 1) + 4 * (u >> 1);
        dma16(rW, lds + 65536 + ((t & 1) * 2 + s) * 16384 + mt * 8192 + (i + 2 * hi2) * 1024, voffB, sb + i * 4 * 7168); }
    };
    stageA(0, 0); stageB(0, 0); stageA(1, 0); stageB(1, 0); stageB(0, 1); stageA(0, 1);
    WAITV(16);
    BAR();
    for (int t = 0; t < 16; ++t) {
      stageA(1, t + 1); WAITV(16); BAR();
      stageB(1, t + 1); WAITV(16); BAR();
      stageB(0, t + 2); WAITV(16); BAR();
      stageA(0, t + 2); WAITV(16); BAR();
    }
    return;
  }
  const int grp = wid >> 2, wrl = (wid >> 1) & 1, wc = wid & 1;
  if (tid == 0) flags[blockIdx.x * 64] = 0u;
  { f32x4 sv[4];
    const float* sp = stats + ((size_t)(rt * 256 + (tid >> 1)) * 16 + (tid & 1) * 8) * 2;
#pragma unroll
    for (int i = 0; i < 4; ++i) sv[i] = *(const f32x4*)(sp + 4 * i);
    const int ci = tid < 224 ? tid : 0; const float csv = cs[3072 + (ci >= 112 ? F_ - 112 : 0) + ct * 112 + ci];
    const f32x2 mr = combine_stats(sv);
    if (!(tid & 1)) *(LAS f32x2*)(lds + K3_STATS + (tid >> 1) * 8) = mr;
    if (tid < 224) *(LAS float*)(lds + K3_CS + tid * 4) = csv; }
  f32x4 acc[4][7];
  {
    const int m = lane & 15, g = lane >> 4, q = (lane >> 2) & 3, p = lane & 3;
    int xa[2], wa[7];
#pragma unroll
    for (int s = 0; s < 2; ++s) xa[s] = grp * 32768 + (wrl * 64 + m) * 128 + (((4 * s + g) ^ (m & 6)) * 16);
    { const int f2 = 2 * (q | ((g & 1) << 2)), ro = 65536 + (8 * g + q) * 256, c0 = 7 * wc;
#pragma unroll
      for (int mm = 0; mm < 2; ++mm) {
#pragma unroll
        for (int b = 0; b < 2; ++b) wa[3 * mm + b] = ro + mm * 8192 + (((c0 + p) ^ f2 ^ mm) * 16) + ((b ^ (p >> 1)) * 8);
        wa[3 * mm + 2] = ro + mm * 8192 + (((c0 + 4 + (p >> 1)) ^ f2 ^ mm) * 16) + (p & 1) * 8;
      }
      wa[6] = ro + (p >> 1) * 8192 + (((c0 + 6) ^ f2 ^ (p >> 1)) * 16) + (p & 1) * 8; }
#pragma unroll
    for (int a = 0; a < 4; ++a)
#pragma unroll
      for (int b = 0; b < 7; ++b) acc[a][b] = (f32x4){0.f, 0.f, 0.f, 0.f};
    WAITL0();
    BAR();
    STAMP_AT(1);
    if (grp == 1) BAR();
    for (int t2 = 0; t2 < 16; t2 += 2) {
#pragma unroll
      for (int buf = 0; buf < 2; ++buf) {
#pragma unroll
        for (int s = 0; s < 2; ++s) {
          bf16x8 xf[4], wf[7];
#pragma unroll
          for (int mb = 0; mb < 4; ++mb) xf[mb] = ld128(lds + xa[s] + buf * 16384 + mb * 2048);
#pragma unroll
          for (int T = 0; T < 7; ++T) wf[T] = ldtr(lds + wa[T] + (buf * 2 + s) * 16384, 1024);
          WAITL0();
          BAR();
          __builtin_amdgcn_s_setprio(1);
#pragma unroll
          for (int mb = 0; mb < 4; ++mb)
#pragma unroll
            for (int T = 0; T < 7; ++T) acc[mb][T] = mfma(wf[T], xf[mb], acc[mb][T]);
          __builtin_amdgcn_s_setprio(0);
          if (buf == 0 || s == 0 || t2 < 14 || grp == 0) BAR();
        }
      }
    }
  }
  STAMP_AT(2);
  int tz = threadIdx.x; asm volatile("" : "+v"(tz));
  const int m = tz & 15, g = (tz >> 4) & 3;
  const LAS f32x2* stt = (const LAS f32x2*)(lds + K3_STATS);
  const LAS float* csl = (const LAS float*)(lds + K3_CS);
  f32x4 c4[7];
#pragma unroll
  for (int mm = 0; mm < 2; ++mm) {
#pragma unroll
    for (int b = 0; b < 2; ++b) c4[3 * mm + b] = *(const LAS f32x4*)(csl + mm * 112 + wc * 56 + 8 * g + 4 * (b ^ (g >> 1)));
    c4[3 * mm + 2] = *(const LAS f32x4*)(csl + mm * 112 + wc * 56 + 32 + 4 * g);
  }
  c4[6] = *(const LAS f32x4*)(csl + (g >> 1) * 112 + wc * 56 + 48 + 4 * (g & 1));
  const bool hi = g >= 2;
  const int mblk0 = rt * 16 + grp * 8 + wrl * 4, G0 = 7 * (2 * ct + wc);
  char* hb = (char*)h + (size_t)mblk0 * 114688 + G0 * 256 + m * 16;
  f32x4 zv[4];
#pragma unroll
  for (int mb = 0; mb < 4; ++mb) {
    const f32x2 mr = stt[grp * 128 + wrl * 64 + mb * 16 + m];
    u32x2 hv[3];
#pragma unroll
    for (int b = 0; b < 3; ++b) {
      const f32x4 gg = (acc[mb][b] - mr[0] * c4[b]) * mr[1], uu = (acc[mb][3 + b] - mr[0] * c4[3 + b]) * mr[1], hh = gg * uu;
      hv[b][0] = pk2(hh[0], hh[1]); hv[b][1] = pk2(hh[2], hh[3]);
    }
    zv[mb] = (acc[mb][6] - mr[0] * c4[6]) * mr[1];
    const u32x2 a = hi ? hv[1] : hv[0], b = hi ? hv[0] : hv[1];
    const u32x4 o = {a[0], a[1], b[0], b[1]};
    *(u32x4*)(hb + (size_t)mb * 114688 + g * 256) = o;
    *(u32x2*)(hb + (size_t)mb * 114688 + (4 + (g >> 1)) * 256 + (g & 1) * 8) = hv[2];
  }
#pragma unroll
  for (int mp = 0; mp < 2; ++mp) {
    float hh[4];
#pragma unroll
    for (int e = 0; e < 4; ++e) {
      const auto r = __builtin_amdgcn_permlane32_swap(__float_as_uint(zv[2 * mp][e]), __float_as_uint(zv[2 * mp + 1][e]), false, false);
      hh[e] = __uint_as_float(r[0]) * __uint_as_float(r[1]);
    }
    const u32x2 o = {pk2(hh[0], hh[1]), pk2(hh[2], hh[3])};
    *(u32x2*)(hb + (size_t)(2 * mp + (g >> 1)) * 114688 + 6 * 256 + (g & 1) * 8) = o;
  }
  STAMP_END(slot);
}

extern "C" void kernel_launch(void* const* d_in, const int* in_sizes, int n_in, void* d_out, int out_size, void* d_ws, size_t ws_size, hipStream_t stream) {
  const u16* x0 = (const u16*)d_in[0];
  const u16* wqkv = (const u16*)d_in[1];
  const u16* wo = (const u16*)d_in[2];
  const u16* wg = (const u16*)d_in[4];
  const u16* wu = (const u16*)d_in[5];
  const u16* wd = (const u16*)d_in[6];
  char* ws = (char*)d_ws;
  u16* pq = (u16*)(ws);
  u16* xa = (u16*)(ws + (4 << 20));
  u16* xb = (u16*)(ws + (8 << 20));
  u16* xc = (u16*)(ws + (12 << 20));
  u16* hbuf = (u16*)(ws + (16 << 20));
  float* kvp = (float*)(ws + (32 << 20));
  unsigned* flags = (unsigned*)(ws + (35 << 20));
  float* st = (float*)(ws + (36 << 20));
  float* cs = (float*)(ws + (38 << 20));
  float* xch = (float*)(ws + (40 << 20));
  (void)hipFuncSetAttribute((const void*)k1_qkv, hipFuncAttributeMaxDynamicSharedMemorySize, K1_LDS);
  (void)hipFuncSetAttribute((const void*)k3_gateup, hipFuncAttributeMaxDynamicSharedMemorySize, K3_LDS);
  (void)hipFuncSetAttribute((const void*)k2_proj, hipFuncAttributeMaxDynamicSharedMemorySize, K2_LDS);
  (void)hipFuncSetAttribute((const void*)k4_down, hipFuncAttributeMaxDynamicSharedMemorySize, K4_LDS);
  k0_prep<<<288, 256, 0, stream>>>(x0, wqkv, wg, wu, st, cs STAMP_PASS(0));
  const u16* xin[2] = {x0, xb};
  u16* xmid[2] = {xa, xc};
  u16* xo[2] = {xb, (u16*)d_out};
  for (int l = 0; l < 2; ++l) {
    float* s0 = st + (size_t)(2 * l) * 65536; float* s1 = s0 + 65536; float* s2 = st + (size_t)((2 * l + 2) & 3) * 65536;
    k1_qkv<<<256, 512, K1_LDS, stream>>>(xin[l], wqkv, s0, cs, pq, kvp STAMP_PASS(1 + 5 * l));
    k1b_o<<<256, 256, 0, stream>>>(kvp, pq STAMP_PASS(2 + 5 * l));
    k2_proj<<<256, 512, K2_LDS, stream>>>(pq, wo, xin[l], xmid[l], s1 STAMP_PASS(3 + 5 * l));
    k3_gateup<<<256, 768, K3_LDS, stream>>>(xmid[l], wg, wu, s1, cs, hbuf, flags STAMP_PASS(4 + 5 * l));
    k4_down<<<256, 512, K4_LDS, stream>>>(hbuf, wd, xmid[l], xo[l], s2, xch, flags STAMP_PASS(5 + 5 * l));
  }
}
```

```cpp
#include <hip/hip_runtime.h>
#include <hip/hip_bf16.h>
#include <stdint.h>

#define LAS __attribute__((address_space(3)))
#define DI __device__ __forceinline__
typedef unsigned short u16;
typedef __bf16 bf16x8 __attribute__((ext_vector_type(8)));
typedef __bf16 bf16x2 __attribute__((ext_vector_type(2)));
typedef short s16x4 __attribute__((ext_vector_type(4)));
typedef float f32x2 __attribute__((ext_vector_type(2)));
typedef float f32x4 __attribute__((ext_vector_type(4)));
typedef unsigned u32x2 __attribute__((ext_vector_type(2)));
typedef unsigned u32x4 __attribute__((ext_vector_type(4)));
typedef u32x4 rsrc_t;
typedef __amdgpu_buffer_rsrc_t brs_t;

constexpr int S_ = 2048, D_ = 1024, F_ = 3584;
#define MEGA 1
#define XCH_BF16 0
#define ROT_K4B 1
#define K0_GRID 768
#define K0_NT 1
#define PF_GU 0
#define PF_ON 1
#define POLL_SLEEP 1
#define POLL_PIPE 0
#define ROT_K1 0
#define ROT_K3 0
#define ROT_K2 0
#define ROT_K4 1
constexpr unsigned OOB_V = 0x80000000u;
constexpr unsigned WS_PQ = 0, WS_KVP = 4u << 20, WS_X1 = 8u << 20, WS_H = 12u << 20, WS_XCH = 27u << 20, WS_ST = 35u << 20, WS_CS = 36u << 20, WS_CNT = (36u << 20) + (1u << 19),
                   WS_BYTES = 38u << 20;
#define NREP 8
constexpr int C_X = 0, C_KV = 16, C_O = 32, C_X1 = 40, C_H = 48, C_FLAG = 128 * NREP, C_N = C_FLAG + 512;
constexpr int L_STATS = 147456, L_CS = L_STATS + 2048, L_KV = L_CS + 1024, L_SYNC = L_KV + 4224, L_BYTES = L_SYNC + 128;

struct Params { const u16* x0; const u16* wqkv; const u16* wo; const u16* wg; const u16* wu; const u16* wd; u16* out; char* ws; };

DI rsrc_t mk_rsrc(const void* p, unsigned bytes) {
  const unsigned long long a = (unsigned long long)p;
  rsrc_t r = {(unsigned)a, (unsigned)(a >> 32) & 0xffffu, bytes, 0x00020000u};
  return r;
}
DI brs_t mk_brs(const void* p, unsigned bytes) { return __builtin_amdgcn_make_buffer_rsrc((void*)p, 0, bytes, 0x00020000); }
DI void dma16(rsrc_t r, LAS char* dst, unsigned voff, unsigned soff) {
  asm volatile("s_mov_b32 m0, %0\n\ts_nop 0\n\tbuffer_load_dwordx4 %1, %2, %3 offen lds" :: "s"((unsigned)(size_t)dst), "v"(voff), "s"(r), "s"(soff) : "memory");
}
DI void dma16c(rsrc_t r, LAS char* dst, unsigned voff, unsigned soff) {
  asm volatile("s_mov_b32 m0, %0\n\ts_nop 0\n\tbuffer_load_dwordx4 %1, %2, %3 offen sc1 lds" :: "s"((unsigned)(size_t)dst), "v"(voff), "s"(r), "s"(soff) : "memory");
}
DI u32x4 ldc16(brs_t r, unsigned voff, unsigned soff) { return __builtin_amdgcn_raw_buffer_load_b128(r, voff, soff, 16); }
DI void stc16(brs_t r, unsigned voff, unsigned soff, u32x4 v) { __builtin_amdgcn_raw_buffer_store_b128(v, r, voff, soff, 16); }
DI void stc16(brs_t r, unsigned voff, unsigned soff, f32x4 v) { stc16(r, voff, soff, __builtin_bit_cast(u32x4, v)); }
DI void stc8(brs_t r, unsigned voff, unsigned soff, u32x2 v) { __builtin_amdgcn_raw_buffer_store_b64(v, r, voff, soff, 16); }
DI f32x4 mfma(bf16x8 a, bf16x8 b, f32x4 c) { return __builtin_amdgcn_mfma_f32_16x16x32_bf16(a, b, c, 0, 0, 0); }
DI bf16x8 ld128(LAS char* p) { return *(LAS bf16x8*)p; }
DI bf16x8 ldtr(LAS char* p, int off2) {
  s16x4 a = __builtin_amdgcn_ds_read_tr16_b64_v4i16((LAS s16x4*)p);
  s16x4 b = __builtin_amdgcn_ds_read_tr16_b64_v4i16((LAS s16x4*)(p + off2));
  return __builtin_bit_cast(bf16x8, __builtin_shufflevector(a, b, 0, 1, 2, 3, 4, 5, 6, 7));
}
DI unsigned pk2(float a, float b) { f32x2 v = {a, b}; return __builtin_bit_cast(unsigned, __builtin_convertvector(v, bf16x2)); }
DI float bflo(unsigned u) { return __uint_as_float(u << 16); }
DI float bfhi(unsigned u) { return __uint_as_float(u & 0xffff0000u); }
DI float phi_ln(float u, float rs, float rs2) {
  float e; asm("v_exp_f32 %0, %1 clamp" : "=v"(e) : "v"(u * rs2));
  return fmaxf(__builtin_fmaf(u, rs, 1e-6f), 1e-6f) + e;
}
DI float xsum(float s) {
  const auto a = __builtin_amdgcn_permlane16_swap(__float_as_uint(s), __float_as_uint(s), false, false);
  s = __uint_as_float(a[0]) + __uint_as_float(a[1]);
  const auto b = __builtin_amdgcn_permlane32_swap(__float_as_uint(s), __float_as_uint(s), false, false);
  return __uint_as_float(b[0]) + __uint_as_float(b[1]);
}
#define SGB(mask, n) __builtin_amdgcn_sched_group_barrier(mask, n, 0)
#define SB0() __builtin_amdgcn_sched_barrier(0)
#define PIN(x) asm volatile("" : "+v"(x))
#define FENCE() asm volatile("" ::: "memory")
#define BAR() do { FENCE(); __builtin_amdgcn_s_barrier(); FENCE(); __builtin_amdgcn_sched_barrier(0); } while (0)
#define WAITV(n) asm volatile("s_waitcnt vmcnt(%0)" :: "i"(n) : "memory")
#define WAITL0() do { FENCE(); __builtin_amdgcn_s_waitcnt(0xc07f); FENCE(); } while (0)
DI int fresh_s(int x) { asm volatile("" : "+s"(x)); return x; }
DI int fresh_lane() { int t; asm volatile("v_mbcnt_lo_u32_b32 %0, -1, 0\n\tv_mbcnt_hi_u32_b32 %0, -1, %0" : "=v"(t)); return t; }

#define STAMP_DECL
#define STAMP_AT(i)
#define STAMP_END(sl_)

template <int N> DI void pf_lines(const void* p) {
#pragma unroll
  for (int i = 0; i < 2 * N; ++i) asm volatile("s_load_dword s100, %0, %1" :: "s"(p), "i"(i * 64) : "s100");
}
DI unsigned* cnt_ptr(char* ws, int idx) { return (unsigned*)(ws + WS_CNT + idx * 128); }
DI unsigned* rep_ptr(char* ws, int idx) { return cnt_ptr(ws, idx * NREP + ((blockIdx.x >> 3) + (blockIdx.x & 7) * 5) % NREP); }
DI void poll_ge(unsigned* c, unsigned target) {
  unsigned spins = 0;
  while ((unsigned)__builtin_amdgcn_readfirstlane(__hip_atomic_load(c, __ATOMIC_RELAXED, __HIP_MEMORY_SCOPE_AGENT)) < target && ++spins < (1u << 18)) __builtin_amdgcn_s_sleep(POLL_SLEEP);
  FENCE();
}
DI void l_wait(char* ws, int idx, unsigned target, LAS char* lds, unsigned epoch, int j) {
  if (!MEGA) return;
  volatile LAS unsigned* relay = (volatile LAS unsigned*)(lds + L_SYNC);
  if (j == 0) { poll_ge(rep_ptr(ws, idx), target); *relay = epoch; }
  else { unsigned spins = 0; while ((unsigned)__builtin_amdgcn_readfirstlane(*relay) < epoch && ++spins < (1u << 21)) __builtin_amdgcn_s_sleep(1); }
  FENCE();
}
template <bool REP> DI void c_signal(char* ws, int idx, LAS char* lds, int arr, unsigned nw) {
  if (!MEGA) return;
  WAITV(0);
  const int lane = fresh_lane();
  unsigned old = 0;
  if (lane == 0) old = __hip_atomic_fetch_add((LAS unsigned*)(lds + L_SYNC + 4 + arr * 4), 1u, __ATOMIC_RELAXED, __HIP_MEMORY_SCOPE_WORKGROUP);
  if ((unsigned)__builtin_amdgcn_readfirstlane(old) == nw - 1 && lane < (REP ? NREP : 1))
    __hip_atomic_fetch_add(cnt_ptr(ws, REP ? idx * NREP + lane : idx), 1u, __ATOMIC_RELAXED, __HIP_MEMORY_SCOPE_AGENT);
}

template <int NT, bool PAIR> DI f32x2 combine_stats(const u32x2 (&u)[NT]) {
  float s = 0.f;
#pragma unroll
  for (int i = 0; i < NT; ++i) s += __uint_as_float(u[i][0]);
  if (PAIR) s += __shfl_xor(s, 1);
  const float mean = s * (1.f / 1024.f);
  float q = 0.f;
#pragma unroll
  for (int i = 0; i < NT; ++i) { const float d = __uint_as_float(u[i][0]) * (1.f / 64.f) - mean; q += __uint_as_float(u[i][1]) + 64.f * d * d; }
  if (PAIR) q += __shfl_xor(q, 1);
  f32x2 r; r[0] = mean; r[1] = 1.0f / sqrtf(q * (1.f / 1024.f) + 1e-5f);
  return r;
}
DI u32x4 ones_bf16() { unsigned o = 0x3f803f80u; asm volatile("" : "+v"(o)); const u32x4 r = {o, o, o, o}; return r; }
DI void res_unpack(f32x4 (&a)[4], const u32x4 (&res)[2], bool hi, bool add) {
#pragma unroll
  for (int pr = 0; pr < 2; ++pr) {
    const unsigned u0 = hi ? res[pr][2] : res[pr][0], u1 = hi ? res[pr][3] : res[pr][1], u2 = hi ? res[pr][0] : res[pr][2], u3 = hi ? res[pr][1] : res[pr][3];
    const f32x4 x = {bflo(u0), bfhi(u0), bflo(u1), bfhi(u1)}, y = {bflo(u2), bfhi(u2), bflo(u3), bfhi(u3)};
    if (add) { a[2 * pr] += x; a[2 * pr + 1] += y; } else { a[2 * pr] = x; a[2 * pr + 1] = y; }
  }
}
DI void x_store(const f32x4 (&a)[4], brs_t rxo, unsigned xoff, brs_t rws, unsigned stoff, int m, int g) {
  const bool hi = g >= 2;
  const u32x4 ones4 = ones_bf16();
  f32x4 sm = {0.f, 0.f, 0.f, 0.f}, sq = {0.f, 0.f, 0.f, 0.f};
#pragma unroll
  for (int pr = 0; pr < 2; ++pr) {
    const unsigned p0 = pk2(a[2 * pr][0], a[2 * pr][1]), p1 = pk2(a[2 * pr][2], a[2 * pr][3]), p2 = pk2(a[2 * pr + 1][0], a[2 * pr + 1][1]), p3 = pk2(a[2 * pr + 1][2], a[2 * pr + 1][3]);
    const u32x4 o = {hi ? p2 : p0, hi ? p3 : p1, hi ? p0 : p2, hi ? p1 : p3};
    stc16(rxo, xoff, pr * 64, o);
    const bf16x8 f = __builtin_bit_cast(bf16x8, o);
    sm = mfma(__builtin_bit_cast(bf16x8, ones4), f, sm);
    sq = mfma(f, f, sq);
  }
  const float dq = (m & 2) ? ((m & 1) ? sq[3] : sq[2]) : ((m & 1) ? sq[1] : sq[0]);
  if (g == (m >> 2)) { const u32x2 r = {__float_as_uint(sm[0]), __float_as_uint(fmaxf(dq - sm[0] * sm[0] * (1.f / 64.f), 0.f))}; stc8(rws, stoff, 0, r); }
}
template <int N, int NI> DI void drain_bar() { WAITV(N * NI); BAR(); if constexpr (N > 0) drain_bar<N - 1, NI>(); }

__global__ __launch_bounds__(256) void k0_prep(Params P) {
  __shared__ float red[32][65];
  STAMP_DECL STAMP_AT(0); STAMP_AT(1); STAMP_AT(2);
  const int b = blockIdx.x, t = threadIdx.x;
  float* stats = (float*)(P.ws + WS_ST); float* cs = (float*)(P.ws + WS_CS);
  if (b * 256 + t < C_N) *cnt_ptr(P.ws, b * 256 + t) = 0u;
  if (b < 128) {
    const int row = b * 16 + (t >> 4), tile = t & 15;
    const u32x4* p = (const u32x4*)(P.x0 + (size_t)row * D_ + tile * 64);
    u32x4 v[8];
#pragma unroll
    for (int i = 0; i < 8; ++i) v[i] = p[i];
    float s = 0.f;
#pragma unroll
    for (int i = 0; i < 8; ++i)
#pragma unroll
      for (int j = 0; j < 4; ++j) s += bflo(v[i][j]) + bfhi(v[i][j]);
    const float m = s * (1.f / 64.f); float q = 0.f;
#pragma unroll
    for (int i = 0; i < 8; ++i)
#pragma unroll
      for (int j = 0; j < 4; ++j) { const float a = bflo(v[i][j]) - m, c = bfhi(v[i][j]) - m; q += a * a + c * c; }
    f32x2 r = {s, q};
    *(f32x2*)(stats + ((size_t)tile * 2048 + row) * 2) = r;
  } else if (b < K0_GRID) {
    const int pn = (b - 128) >> 2, kq = (b - 128) & 3;
    const u16* w; int ld, col, outoff;
    if (pn < 48) { w = P.wqkv; ld = 3072; col = pn * 64; outoff = col; }
    else if (pn < 104) { w = P.wg; ld = F_; col = (pn - 48) * 64; outoff = 3072 + col; }
    else { w = P.wu; ld = F_; col = (pn - 104) * 64; outoff = 3072 + F_ + col; }
    const int c8 = t & 7, r0 = t >> 3;
    const u32x4* src = (const u32x4*)(w + (size_t)(kq * 256 + r0) * ld + col + c8 * 8);
    u32x4 v[8];
    if (K0_NT == 2 || (K0_NT && pn >= 48)) {
#pragma unroll
      for (int i = 0; i < 8; ++i) v[i] = __builtin_nontemporal_load(src + (size_t)i * 4 * ld);
    } else {
#pragma unroll
      for (int i = 0; i < 8; ++i) v[i] = src[(size_t)i * 4 * ld];
    }
    float a[8] = {0, 0, 0, 0, 0, 0, 0, 0};
#pragma unroll
    for (int i = 0; i < 8; ++i)
#pragma unroll
      for (int j = 0; j < 4; ++j) { a[2 * j] += bflo(v[i][j]); a[2 * j + 1] += bfhi(v[i][j]); }
#pragma unroll
    for (int j = 0; j < 8; ++j) red[r0][c8 * 8 + j] = a[j];
    __syncthreads();
    if (t < 64) { float s = 0.f;
#pragma unroll
      for (int r = 0; r < 32; ++r) s += red[r][t];
      cs[(outoff + t) * 4 + kq] = s; }
  }
#define STAMP_WID __builtin_amdgcn_readfirstlane(threadIdx.x >> 6)
  STAMP_END(0);
#undef STAMP_WID
#define STAMP_WID wid
}

#define ALOAD8C(dst, rs, voff, soff) asm volatile("buffer_load_dwordx2 %0, %1, %2, %3 offen sc1" : "=v"(dst) : "v"(voff), "s"(rs), "s"(soff) : "memory")
#define ALOAD16(dst, rs, voff, soff) asm volatile("buffer_load_dwordx4 %0, %1, %2, %3 offen" : "=v"(dst) : "v"(voff), "s"(rs), "s"(soff) : "memory")

constexpr int K1_STAGE = 40960, K1_RED = 49152;
DI void p1_load(const Params& P, int l, LAS char* lds, int j) {
  const int lane = fresh_lane(), tid = j * 64 + lane;
  const int bid = fresh_s(blockIdx.x), xcd = bid & 7, loc = bid >> 3;
  const int rt = (xcd & 1) * 8 + (loc & 7), hp = (xcd >> 1) * 4 + (loc >> 3);
  const int rot = ROT_K1 ? ((loc & 7) + 2 * (loc >> 3)) & 7 : 0;
  const rsrc_t rX = mk_rsrc(l ? P.out : P.x0, S_ * D_ * 2), rW = mk_rsrc(P.wqkv, D_ * 3072 * 2), rS = mk_rsrc(P.ws, WS_BYTES);
  const int r = lane >> 3, sl8 = lane & 7, f = ((r >> 1) & 1) | ((j & 1) << 1);
  const unsigned voffX = r * 2048 + ((sl8 ^ (r & 6)) * 16), voffW = r * 6144 + ((sl8 ^ (2 * f)) * 16);
  auto stX = [&](int t, int st) {
    const unsigned sx = (unsigned)((rt * 128 + j * 32) * 2048 + ((t & 8) | ((t + rot) & 7)) * 128);
#pragma unroll
    for (int u = 0; u < 4; ++u) dma16c(rX, lds + st * K1_STAGE + (4 * j + u) * 1024, voffX, sx + u * 16384);
  };
  auto stW = [&](int t, int st) {
    const unsigned sw = (unsigned)((((t & 8) | ((t + rot) & 7)) * 64 + j * 8) * 6144 + hp * 128);
#pragma unroll
    for (int s = 0; s < 2; ++s)
#pragma unroll
      for (int pnl = 0; pnl < 3; ++pnl) dma16(rW, lds + st * K1_STAGE + 16384 + pnl * 8192 + (4 * s + j) * 1024, voffW, sw + pnl * 2048 + s * 32 * 6144);
  };
  stW(0, 0); stW(1, 1);
  if (l) l_wait(P.ws, 64 + C_X + rt, 16, lds, 5, j);
  stX(0, 0); stX(1, 1);
  u32x2 sv[8]; f32x4 csv;
  { const int t = tid;
    const unsigned so = WS_ST + (unsigned)(2 * l) * 262144u + rt * 1024, vo = (t & 1) * 131072 + (t >> 1) * 8;
#pragma unroll
    for (int i = 0; i < 8; ++i) ALOAD8C(sv[i], rS, vo, so + i * 16384);
    const int ci = t < 192 ? t : 0;
    ALOAD16(csv, rS, ((ci >> 6) * 1024 + (ci & 63)) * 16, WS_CS + hp * 1024); }
  WAITV(13); BAR();
  stX(2, 2); stW(2, 2);
  WAITV(19); BAR();
  stX(3, 0); stW(3, 0);
  WAITV(10); BAR();
  stX(4, 1); stW(4, 1);
  { const int t = tid;
#pragma unroll
    for (int i = 0; i < 8; ++i) PIN(sv[i]);
    PIN(csv);
    const f32x2 mr = combine_stats<8, true>(sv);
    if (!(t & 1)) *(LAS f32x2*)(lds + L_STATS + (t >> 1) * 8) = mr;
    if (t < 192) *(LAS float*)(lds + L_CS + t * 4) = (csv[0] + csv[1]) + (csv[2] + csv[3]);
    WAITL0(); }
  WAITV(10); BAR();
  int st = 2;
  for (int t = 3; t < 14; ++t) {
    stX(t + 2, st); stW(t + 2, st);
    if (l == 0 && t < 11) pf_lines<2>((const char*)P.wo + ((bid * 4 + j) * 16 + (t - 3) * 2) * 128);
    if (PF_GU && l == 0 && t < 13) pf_lines<PF_GU>((const char*)((j & 1) ? P.wu : P.wg) + ((bid * 2 + (j >> 1)) * 112 + (t - 3) * PF_GU) * 128);
    WAITV(10); BAR();
    st = st == 2 ? 0 : st + 1;
  }
  WAITV(0); BAR();
  BAR();
  BAR(); BAR();
}
DI void p1_comp(const Params& P, int l, LAS char* lds, int wid) {
  STAMP_DECL STAMP_AT(0);
  const int bid = fresh_s(blockIdx.x), xcd = bid & 7, loc = bid >> 3;
  const int rt = (xcd & 1) * 8 + (loc & 7), hp = (xcd >> 1) * 4 + (loc >> 3);
  if (wid >= 4) {
#pragma unroll 1
    for (int i = 0; i < 19; ++i) BAR();
    STAMP_AT(0);
    volatile LAS unsigned* relay = (volatile LAS unsigned*)(lds + L_SYNC + 56);
    if (wid == 4) { poll_ge(rep_ptr(P.ws, 64 * l + C_KV + hp), 16); *relay = l + 1; }
    else { unsigned spins = 0; while ((unsigned)__builtin_amdgcn_readfirstlane(*relay) < (unsigned)(l + 1) && ++spins < (1u << 21)) __builtin_amdgcn_s_sleep(1); }
    FENCE();
    STAMP_AT(1);
    const int lane = fresh_lane(), hw = (wid >> 1) & 1, wr = wid & 1;
    const brs_t rws = mk_brs(P.ws, WS_BYTES);
    const unsigned src = WS_KVP + (2 * hp + hw) * 2176;
    u32x4 v[16];
#pragma unroll
    for (int i = 0; i < 16; ++i) v[i] = ldc16(rws, lane * 16, src + i * 32 * 2176 + wr * 1024);
    const u32x2 zv = __builtin_amdgcn_raw_buffer_load_b64(rws, (lane >> 2) * (32 * 2176) + (lane & 3) * 8, src + 2048 + wr * 32, 16);
    float s[8] = {0.f, 0.f, 0.f, 0.f, 0.f, 0.f, 0.f, 0.f};
#pragma unroll
    for (int i = 0; i < 16; ++i)
#pragma unroll
      for (int e = 0; e < 4; ++e) { s[2 * e] += bflo(v[i][e]); s[2 * e + 1] += bfhi(v[i][e]); }
#pragma unroll
    for (int b = 0; b < 2; ++b) {
      const int e = 16 * b + (lane & 15), i16 = (e >> 3) * 4 + (e & 3), bp = (e >> 2) & 1;
      const u32x2 o = {pk2(s[4 * b], s[4 * b + 1]), pk2(s[4 * b + 2], s[4 * b + 3])};
      *(LAS u32x2*)(lds + L_KV + hw * 2112 + bp * 1024 + ((lane >> 4) * 16 + i16) * 16 + 8 * wr) = o;
    }
    float z[4] = {bflo(zv[0]), bfhi(zv[0]), bflo(zv[1]), bfhi(zv[1])};
#pragma unroll
    for (int e = 0; e < 4; ++e) {
      z[e] += __uint_as_float(__builtin_amdgcn_update_dpp(0u, __float_as_uint(z[e]), 0x124, 0xf, 0xf, false));
      z[e] += __uint_as_float(__builtin_amdgcn_update_dpp(0u, __float_as_uint(z[e]), 0x128, 0xf, 0xf, false));
      z[e] = xsum(z[e]);
    }
    if (lane < 4) { const u32x2 oz = {pk2(z[0], z[1]), pk2(z[2], z[3])}; *(LAS u32x2*)(lds + L_KV + hw * 2112 + 2048 + lane * 16 + 8 * wr) = oz; }
    WAITL0();
    BAR();
    return;
  }
  const int wr = wid >> 1, wc = wid & 1;
  f32x4 acc[3][4][2];
  {
    const int lane = fresh_lane(), m = lane & 15, g = lane >> 4, q = (lane >> 2) & 3, p = lane & 3;
    int xa0 = (wr * 64 + m) * 128 + ((g ^ (m & 6)) * 16), xa1 = xa0 ^ 64;
    int wa0 = 16384 + (8 * g + q) * 128 + (((4 * wc + (p >> 1)) ^ (2 * (((q >> 1) & 1) | ((g & 1) << 1)))) * 16) + (p & 1) * 8, wa1 = wa0 ^ 32;
#pragma unroll
    for (int a = 0; a < 3; ++a)
#pragma unroll
      for (int b = 0; b < 4; ++b)
#pragma unroll
        for (int c = 0; c < 2; ++c) acc[a][b][c] = (f32x4){0.f, 0.f, 0.f, 0.f};
    bf16x8 xf[4], wf[2][3][2];
    auto step = [&](int cur, bool bar) {
      constexpr bool more = true;
      LAS char* xp = lds + (cur ? xa0 : xa1); LAS char* w0 = lds + wa0 + (cur ? 0 : 4096); LAS char* w1 = lds + wa1 + (cur ? 0 : 4096);
#pragma unroll
      for (int mb = 0; mb < 4; ++mb) {
        const bf16x8 x = xf[mb];
        if (more && mb == 1) { wf[cur ^ 1][0][0] = ldtr(w0, 512); xf[0] = ld128(xp); wf[cur ^ 1][1][0] = ldtr(w0 + 8192, 512); }
        if (more && mb == 2) { wf[cur ^ 1][2][0] = ldtr(w0 + 16384, 512); xf[1] = ld128(xp + 2048); wf[cur ^ 1][0][1] = ldtr(w1, 512); }
        if (more && mb == 3) { wf[cur ^ 1][1][1] = ldtr(w1 + 8192, 512); xf[2] = ld128(xp + 4096); wf[cur ^ 1][2][1] = ldtr(w1 + 16384, 512); }
#pragma unroll
        for (int nb = 0; nb < 2; ++nb) {
          acc[0][mb][nb] = mfma(wf[cur][0][nb], x, acc[0][mb][nb]);
          acc[1][mb][nb] = mfma(x, wf[cur][1][nb], acc[1][mb][nb]);
          acc[2][mb][nb] = mfma(x, wf[cur][2][nb], acc[2][mb][nb]);
        }
        if (mb == 0 && cur) { SB0(); if (bar) { WAITL0(); BAR(); } }
        if (more && mb == 3) xf[3] = ld128(xp + 6144);
        if (more && mb > 0) { _Pragma("unroll") for (int i_ = 0; i_ < 5; ++i_) { SGB(0x8, 1); SGB(0x100, 1); } SGB(0x8, 1); SGB(0x100, 1); }
        SB0();
      }
    };
    BAR();
    STAMP_AT(1);
#pragma unroll
    for (int mb = 0; mb < 4; ++mb) xf[mb] = ld128(lds + xa0 + mb * 2048);
#pragma unroll
    for (int pnl = 0; pnl < 3; ++pnl) { wf[0][pnl][0] = ldtr(lds + wa0 + pnl * 8192, 512); wf[0][pnl][1] = ldtr(lds + wa1 + pnl * 8192, 512); }
    SB0();
    int st = 0;
    for (int t = 0; t < 16; ++t) {
      step(0, false);
      const int d = st == 2 ? -2 * K1_STAGE : K1_STAGE; st = st == 2 ? 0 : st + 1;
      xa0 += d; xa1 += d; wa0 += d; wa1 += d; PIN(xa0); PIN(xa1); PIN(wa0); PIN(wa1);
      step(1, true);
    }
  }
  STAMP_AT(2);
  const int lane = fresh_lane(), m = lane & 15, g = lane >> 4;
  const brs_t rws = mk_brs(P.ws, WS_BYTES);
  const int head = 2 * hp + wc, trow = wr * 64;
  const LAS f32x2* stt = (const LAS f32x2*)(lds + L_STATS);
  const LAS float* csl = (const LAS float*)(lds + L_CS);
  const u32x4 ones4 = ones_bf16();
  f32x4 kvr[6];
#pragma unroll
  for (int i = 0; i < 6; ++i) kvr[i] = (f32x4){0.f, 0.f, 0.f, 0.f};
  float ck[2], cv[2];
#pragma unroll
  for (int nb = 0; nb < 2; ++nb) { ck[nb] = csl[64 + wc * 32 + nb * 16 + m]; cv[nb] = csl[128 + wc * 32 + nb * 16 + m]; }
#pragma unroll
  for (int mp = 0; mp < 2; ++mp) {
    u32x4 pkf[2], vf[2];
#pragma unroll
    for (int hb = 0; hb < 2; ++hb) {
      const int mb = 2 * mp + hb;
      const f32x4 a0 = *(const LAS f32x4*)(stt + trow + mb * 16 + 4 * g), a1 = *(const LAS f32x4*)(stt + trow + mb * 16 + 4 * g + 2);
      const float mean[4] = {a0[0], a0[2], a1[0], a1[2]}, rstd[4] = {a0[1], a0[3], a1[1], a1[3]};
#pragma unroll
      for (int nb = 0; nb < 2; ++nb) {
        float kk[4], vv[4];
#pragma unroll
        for (int e = 0; e < 4; ++e) { kk[e] = phi_ln(acc[1][mb][nb][e] - mean[e] * ck[nb], rstd[e], rstd[e] * 1.44269504f); vv[e] = rstd[e] * (acc[2][mb][nb][e] - mean[e] * cv[nb]); }
        pkf[nb][2 * hb] = pk2(kk[0], kk[1]); pkf[nb][2 * hb + 1] = pk2(kk[2], kk[3]);
        vf[nb][2 * hb] = pk2(vv[0], vv[1]); vf[nb][2 * hb + 1] = pk2(vv[2], vv[3]);
      }
      SB0();
    }
#pragma unroll
    for (int a = 0; a < 2; ++a) {
#pragma unroll
      for (int b = 0; b < 2; ++b) kvr[a * 3 + b] = mfma(__builtin_bit_cast(bf16x8, pkf[a]), __builtin_bit_cast(bf16x8, vf[b]), kvr[a * 3 + b]);
      kvr[a * 3 + 2] = mfma(__builtin_bit_cast(bf16x8, pkf[a]), __builtin_bit_cast(bf16x8, ones4), kvr[a * 3 + 2]);
    }
  }
  f32x4 own[3];
#pragma unroll
  for (int i = 0; i < 3; ++i) {
    f32x4 snd;
#pragma unroll
    for (int e = 0; e < 4; ++e) { snd[e] = wr ? kvr[i][e] : kvr[3 + i][e]; own[i][e] = wr ? kvr[3 + i][e] : kvr[i][e]; }
    *(LAS f32x4*)(lds + K1_RED + wid * 3072 + i * 1024 + lane * 16) = snd;
  }
  WAITL0();
  BAR();
#pragma unroll
  for (int i = 0; i < 3; ++i) own[i] += *(LAS f32x4*)(lds + K1_RED + (wid ^ 2) * 3072 + i * 1024 + lane * 16);
  { const unsigned dst = WS_KVP + (rt * 32 + head) * 2176;
    const u32x4 o = {pk2(own[0][0], own[0][1]), pk2(own[0][2], own[0][3]), pk2(own[1][0], own[1][1]), pk2(own[1][2], own[1][3])};
    stc16(rws, lane * 16, dst + wr * 1024, o);
    const u32x2 oz = {pk2(own[2][0], own[2][1]), pk2(own[2][2], own[2][3])};
    if (m == 0) stc8(rws, g * 8, dst + 2048 + wr * 32, oz); }
  BAR();
  c_signal<true>(P.ws, 64 * l + C_KV + hp, lds, 5 * l + 0, 4);
  f32x4 c4[2];
#pragma unroll
  for (int nb = 0; nb < 2; ++nb) c4[nb] = *(const LAS f32x4*)(csl + wc * 32 + nb * 16 + 4 * g);
  u32x4 pq[4];
#pragma unroll
  for (int mb = 0; mb < 4; ++mb) {
    const f32x2 mr = stt[trow + mb * 16 + m];
    const float rs2 = mr[1] * 1.44269504f;
#pragma unroll
    for (int nb = 0; nb < 2; ++nb) {
      f32x4 v;
#pragma unroll
      for (int e = 0; e < 4; ++e) v[e] = phi_ln(acc[0][mb][nb][e] - mr[0] * c4[nb][e], mr[1], rs2);
      pq[mb][2 * nb] = pk2(v[0], v[1]); pq[mb][2 * nb + 1] = pk2(v[2], v[3]);
    }
    PIN(pq[mb]);
  }
  STAMP_END(1 + 5 * l);
  STAMP_AT(0);
  BAR();
  STAMP_AT(1); STAMP_AT(2);
  { LAS char* kb = lds + L_KV + wc * 2112;
    const bf16x8 k0 = ld128(kb + lane * 16), k1 = ld128(kb + 1024 + lane * 16), zf = ld128(kb + 2048 + g * 16);
    const f32x4 z4 = {0.f, 0.f, 0.f, 0.f};
#pragma unroll
    for (int mb = 0; mb < 4; ++mb) {
      const bf16x8 x = __builtin_bit_cast(bf16x8, pq[mb]);
      const f32x4 dn = mfma(zf, x, z4), d0 = mfma(k0, x, z4), d1 = mfma(k1, x, z4);
      const float rr = __builtin_amdgcn_rcpf(dn[0] + 1e-6f);
      const u32x4 o = {pk2(d0[0] * rr, d0[1] * rr), pk2(d0[2] * rr, d0[3] * rr), pk2(d1[0] * rr, d1[1] * rr), pk2(d1[2] * rr, d1[3] * rr)};
      stc16(rws, lane * 16, WS_PQ + ((rt * 8 + wr * 4 + mb) * 32 + head) * 1024, o);
    } }
  c_signal<true>(P.ws, 64 * l + C_O + (rt >> 1), lds, 5 * l + 1, 4);
  STAMP_END(2 + 5 * l);
}

constexpr int K2_NT = 16, K2_STAGE = 24576, K2_NS = 6;
template <bool P1B> DI void p2_load(const Params& P, int l, LAS char* lds, int j) {
  const int lane = fresh_lane();
  const int bid = fresh_s(blockIdx.x), xcd = bid & 7, loc = bid >> 3;
  const int rt = (xcd & 3) * 4 + (loc & 3), ct = (xcd >> 2) * 8 + (loc >> 2);
  const int rot = ROT_K2 ? ((loc >> 2) + 2 * (loc & 3)) & 7 : 0;
  const rsrc_t rA = mk_rsrc(P.ws, WS_BYTES), rW = mk_rsrc(P.wo, D_ * D_ * 2);
  const int r = lane >> 3, sl8 = lane & 7, f = ((r >> 1) & 1) | ((j & 1) << 1);
  const unsigned voffW = r * 2048 + ((sl8 ^ (2 * f)) * 16), voffL = lane * 16;
  auto stX = [&](int t, int sl) {
    const unsigned sa = WS_PQ + (unsigned)((rt * 8 + 2 * j) * 32768 + ((t & ~7) | ((t + rot) & 7)) * 2048);
#pragma unroll
    for (int u = 0; u < 4; ++u) dma16c(rA, lds + sl * K2_STAGE + (4 * j + u) * 1024, voffL, sa + (u >> 1) * 32768 + (u & 1) * 1024);
  };
  auto stW = [&](int t, int sl) {
    const unsigned sw = (unsigned)((((t & ~7) | ((t + rot) & 7)) * 64 + j * 8) * 2048 + ct * 128);
#pragma unroll
    for (int u = 0; u < 2; ++u) dma16(rW, lds + sl * K2_STAGE + 16384 + (j + 4 * u) * 1024, voffW, sw + u * 32 * 2048);
  };
#pragma unroll
  for (int t = 0; t < 5; ++t) stW(t, t);
  if (P1B) BAR();
  l_wait(P.ws, 64 * l + C_O + (rt >> 1), 32, lds, 5 * l + 2, j);
#pragma unroll
  for (int t = 0; t < 5; ++t) stX(t, t);
  WAITV(16); BAR(); stX(5, 5); stW(5, 5);
  WAITV(18); BAR(); stX(6, 0); stW(6, 0);
  WAITV(20); BAR(); stX(7, 1); stW(7, 1);
  WAITV(22); BAR(); stX(8, 2); stW(8, 2);
  int sl = 3;
  for (int t = 4; t < K2_NT - 5; ++t) {
    WAITV(24); BAR();
    stX(t + 5, sl); stW(t + 5, sl);
    if (PF_GU && l == 0) pf_lines<2>((const char*)((j & 1) ? P.wu : P.wg) + ((bid * 2 + (j >> 1)) * 112 + 10 * PF_GU + (t - 4) * 2) * 128);
    sl = sl == K2_NS - 1 ? 0 : sl + 1;
  }
  drain_bar<4, 6>();
  BAR();
}
DI void p2_comp(const Params& P, int l, LAS char* lds, int wid) {
  STAMP_DECL STAMP_AT(0);
  if (wid >= 4) {
#pragma unroll 1
    for (int i = 0; i < K2_NT + 1; ++i) BAR();
    return;
  }
  const int lane = fresh_lane();
  const int bid = fresh_s(blockIdx.x), xcd = bid & 7, loc = bid >> 3;
  const int rt = (xcd & 3) * 4 + (loc & 3), ct = (xcd >> 2) * 8 + (loc >> 2);
  const int m = lane & 15, g = lane >> 4, q = (lane >> 2) & 3, p = lane & 3;
  const int row0 = rt * 128 + wid * 32 + m;
  const unsigned off0 = (unsigned)(row0 * D_ + ct * 64 + 8 * g) * 2;
  int wa[4];
  { const int f = ((q >> 1) & 1) | ((g & 1) << 1);
#pragma unroll
    for (int nb = 0; nb < 4; ++nb) wa[nb] = 16384 + (8 * g + q) * 128 + (((4 * (nb >> 1) + p) ^ (2 * f)) * 16) + (((nb & 1) ^ (p >> 1)) * 8); }
  const int xa = wid * 4096 + lane * 16;
  f32x4 acc[2][4];
  { const brs_t rxi = mk_brs(l ? P.out : P.x0, S_ * D_ * 2);
    u32x4 res[2][2];
#pragma unroll
    for (int mb = 0; mb < 2; ++mb) { res[mb][0] = ldc16(rxi, off0, mb * 32768); res[mb][1] = ldc16(rxi, off0, mb * 32768 + 64); }
#pragma unroll
    for (int mb = 0; mb < 2; ++mb) res_unpack(acc[mb], res[mb], g >= 2, false); }
  bf16x8 xf[2][2][2], wf[2][2][4];
  auto fetch = [&](int set, int sl) {
    LAS char* b = lds + sl * K2_STAGE;
#pragma unroll
    for (int s = 0; s < 2; ++s) {
      xf[set][s][0] = ld128(b + xa + s * 1024); xf[set][s][1] = ld128(b + xa + s * 1024 + 2048);
#pragma unroll
      for (int nb = 0; nb < 4; ++nb) wf[set][s][nb] = ldtr(b + wa[nb] + s * 4096, 512);
    }
  };
  auto mma = [&](int set) {
#pragma unroll
    for (int s = 0; s < 2; ++s)
#pragma unroll
      for (int nb = 0; nb < 4; ++nb)
#pragma unroll
        for (int mb = 0; mb < 2; ++mb) acc[mb][nb] = mfma(wf[set][s][nb], xf[set][s][mb], acc[mb][nb]);
  };
#define K2_MIX() do { _Pragma("unroll") for (int i_ = 0; i_ < 10; ++i_) { SGB(0x8, 1); SGB(0x100, 2); } SGB(0x8, 6); SB0(); } while (0)
  BAR();
  STAMP_AT(1);
  fetch(0, 0);
  SB0();
  int sl = 1;
  for (int t = 1; t < K2_NT - 1; t += 2) {
    WAITL0(); BAR();
    fetch(1, sl); mma(0); K2_MIX();
    sl = sl == K2_NS - 1 ? 0 : sl + 1;
    WAITL0(); BAR();
    fetch(0, sl); mma(1); K2_MIX();
    sl = sl == K2_NS - 1 ? 0 : sl + 1;
  }
  WAITL0(); BAR();
  fetch(1, sl); mma(0); K2_MIX();
  mma(1);
  STAMP_AT(2);
  const brs_t rws = mk_brs(P.ws, WS_BYTES);
#pragma unroll
  for (int mb = 0; mb < 2; ++mb)
    x_store(acc[mb], rws, WS_X1 + off0 + mb * 32768, rws, WS_ST + (unsigned)(2 * l + 1) * 262144u + (ct * 2048 + row0 + mb * 16) * 8, m, g);
  BAR();
  c_signal<true>(P.ws, 64 * l + C_X1 + (rt >> 1), lds, 5 * l + 2, 4);
  STAMP_END(3 + 5 * l);
}

DI void p3_load(const Params& P, int l, LAS char* lds, int j) {
  const int lane = fresh_lane(), tid = j * 64 + lane;
  const int bid = fresh_s(blockIdx.x), xcd = bid & 7, loc = bid >> 3;
  const int rt = (xcd & 1) * 4 + (loc & 3), ct = (xcd >> 1) * 8 + (loc >> 2);
  const int rot = ROT_K3 ? ((loc >> 2) + 2 * (loc & 3)) & 7 : 0;
  const int mt = j & 1, hi2 = j >> 1;
  unsigned voffA, voffB;
  { const int r = lane >> 3, sl8 = lane & 7; voffA = r * 2048 + ((sl8 ^ (r & 6)) * 16);
    const int qq = lane >> 4, sl = lane & 15, f = qq | (hi2 << 2), ch = sl ^ (2 * f) ^ mt; voffB = ch < 14 ? (unsigned)(qq * 7168 + ch * 16) : OOB_V; }
  const rsrc_t rS = mk_rsrc(P.ws, WS_BYTES), rW = mk_rsrc(mt ? P.wu : P.wg, D_ * F_ * 2);
  auto stA = [&](int half, int t) {
    const unsigned sb = WS_X1 + (unsigned)((rt * 256 + half * 128 + j * 32) * 2048 + ((t & 8) | ((t + rot) & 7)) * 128);
#pragma unroll
    for (int u = 0; u < 4; ++u) dma16c(rS, lds + half * 32768 + (t & 1) * 16384 + (4 * j + u) * 1024, voffA, sb + u * 16384);
  };
  auto stB = [&](int s, int t) {
    const unsigned sb = (unsigned)((((t & 8) | ((t + rot) & 7)) * 64 + s * 32 + hi2 * 8) * 7168 + ct * 224);
#pragma unroll
    for (int u = 0; u < 4; ++u) { const int i = (u & 1) + 4 * (u >> 1);
      dma16(rW, lds + 65536 + ((t & 1) * 2 + s) * 16384 + mt * 8192 + (i + 2 * hi2) * 1024, voffB, sb + i * 4 * 7168); }
  };
  stB(0, 0); stB(1, 0); stB(0, 1);
  l_wait(P.ws, 64 * l + C_X1 + rt, 32, lds, 5 * l + 3, j);
  stA(0, 0); stA(1, 0); stA(0, 1);
  u32x2 sv[16]; f32x4 csv;
  { const int t = tid;
    const unsigned so = WS_ST + (unsigned)(2 * l + 1) * 262144u + rt * 2048, vo = t * 8;
#pragma unroll
    for (int i = 0; i < 16; ++i) ALOAD8C(sv[i], rS, vo, so + i * 16384);
    const int ci = t < 224 ? t : 0;
    ALOAD16(csv, rS, (ci + (ci >= 112 ? F_ - 112 : 0)) * 16, WS_CS + (3072 + ct * 112) * 16); }
  WAITV(25); BAR();
  stA(1, 1); WAITV(25); BAR();
  stB(1, 1); WAITV(33); BAR();
  stB(0, 2); WAITV(37); BAR();
  stA(0, 2); WAITV(16); BAR();
  stA(1, 2);
  { const int t = tid;
#pragma unroll
    for (int i = 0; i < 16; ++i) PIN(sv[i]);
    PIN(csv);
    *(LAS f32x2*)(lds + L_STATS + t * 8) = combine_stats<16, false>(sv);
    if (t < 224) *(LAS float*)(lds + L_CS + t * 4) = (csv[0] + csv[1]) + (csv[2] + csv[3]);
    WAITL0(); }
  WAITV(16); BAR();
  stB(1, 2); WAITV(16); BAR();
  stB(0, 3); WAITV(16); BAR();
  stA(0, 3); WAITV(16); BAR();
  for (int t = 2; t < 14; ++t) {
    const char* pfp = (const char*)P.wd + ((bid * 4 + j) * 56 + (t - 2) * 7) * 128;
    const bool pf = l == 0 && t < 10;
    stA(1, t + 1); if (pf) pf_lines<2>(pfp); WAITV(16); BAR();
    stB(1, t + 1); if (pf) pf_lines<2>(pfp + 256); WAITV(16); BAR();
    stB(0, t + 2); if (pf) pf_lines<2>(pfp + 512); WAITV(16); BAR();
    stA(0, t + 2); if (pf) pf_lines<1>(pfp + 768); WAITV(16); BAR();
  }
  stA(1, 15); WAITV(16); BAR();
  stB(1, 15); WAITV(16); BAR();
  drain_bar<3, 4>();
  BAR(); BAR();
  BAR();
}
DI void p3_comp(const Params& P, int l, LAS char* lds, int wid) {
  STAMP_DECL STAMP_AT(0);
  const int bid = fresh_s(blockIdx.x), xcd = bid & 7, loc = bid >> 3;
  const int rt = (xcd & 1) * 4 + (loc & 3), ct = (xcd >> 1) * 8 + (loc >> 2);
  const int grp = wid >> 2, wrl = (wid >> 1) & 1, wc = wid & 1;
  f32x4 acc[4][7];
  {
    const int lane = fresh_lane();
    const int m = lane & 15, g = lane >> 4, q = (lane >> 2) & 3, p = lane & 3;
    int xa[2], wa[7];
#pragma unroll
    for (int s = 0; s < 2; ++s) xa[s] = grp * 32768 + (wrl * 64 + m) * 128 + (((4 * s + g) ^ (m & 6)) * 16);
    { const int f2 = 2 * (q | ((g & 1) << 2)), ro = 65536 + (8 * g + q) * 256, c0 = 7 * wc;
#pragma unroll
      for (int mm = 0; mm < 2; ++mm) {
#pragma unroll
        for (int b = 0; b < 2; ++b) wa[3 * mm + b] = ro + mm * 8192 + (((c0 + p) ^ f2 ^ mm) * 16) + ((b ^ (p >> 1)) * 8);
        wa[3 * mm + 2] = ro + mm * 8192 + (((c0 + 4 + (p >> 1)) ^ f2 ^ mm) * 16) + (p & 1) * 8;
      }
      wa[6] = ro + (p >> 1) * 8192 + (((c0 + 6) ^ f2 ^ (p >> 1)) * 16) + (p & 1) * 8; }
#pragma unroll
    for (int a = 0; a < 4; ++a)
#pragma unroll
      for (int b = 0; b < 7; ++b) acc[a][b] = (f32x4){0.f, 0.f, 0.f, 0.f};
    BAR();
    STAMP_AT(1);
    if (grp == 1) BAR();
    for (int t2 = 0; t2 < 16; t2 += 2) {
#pragma unroll
      for (int buf = 0; buf < 2; ++buf) {
#pragma unroll
        for (int s = 0; s < 2; ++s) {
          bf16x8 xf[4], wf[7];
#pragma unroll
          for (int mb = 0; mb < 4; ++mb) xf[mb] = ld128(lds + xa[s] + buf * 16384 + mb * 2048);
#pragma unroll
          for (int T = 0; T < 7; ++T) wf[T] = ldtr(lds + wa[T] + (buf * 2 + s) * 16384, 1024);
          WAITL0();
          BAR();
          __builtin_amdgcn_s_setprio(1);
#pragma unroll
          for (int mb = 0; mb < 4; ++mb)
#pragma unroll
            for (int T = 0; T < 7; ++T) acc[mb][T] = mfma(wf[T], xf[mb], acc[mb][T]);
          __builtin_amdgcn_s_setprio(0);
          if (buf == 0 || s == 0 || t2 < 14 || grp == 0) BAR();
        }
      }
    }
  }
  STAMP_AT(2);
  const int tz = fresh_lane();
  const int m = tz & 15, g = tz >> 4;
  const LAS f32x2* stt = (const LAS f32x2*)(lds + L_STATS);
  const LAS float* csl = (const LAS float*)(lds + L_CS);
  f32x4 c4[7];
#pragma unroll
  for (int mm = 0; mm < 2; ++mm) {
#pragma unroll
    for (int b = 0; b < 2; ++b) c4[3 * mm + b] = *(const LAS f32x4*)(csl + mm * 112 + wc * 56 + 8 * g + 4 * (b ^ (g >> 1)));
    c4[3 * mm + 2] = *(const LAS f32x4*)(csl + mm * 112 + wc * 56 + 32 + 4 * g);
  }
  c4[6] = *(const LAS f32x4*)(csl + (g >> 1) * 112 + wc * 56 + 48 + 4 * (g & 1));
  const bool hi = g >= 2;
  const brs_t rws = mk_brs(P.ws, WS_BYTES);
  const unsigned hb = WS_H + (unsigned)(rt * 16 + grp * 8 + wrl * 4) * 114688u + (unsigned)(7 * (2 * ct + wc)) * 256u;
  const unsigned v16 = m * 16 + g * 256, v8 = m * 16 + (g >> 1) * 256 + (g & 1) * 8, vz = m * 16 + (g >> 1) * 114688 + (g & 1) * 8;
  f32x4 zv[4];
#pragma unroll
  for (int mb = 0; mb < 4; ++mb) {
    const f32x2 mr = stt[grp * 128 + wrl * 64 + mb * 16 + m];
    u32x2 hv[3];
#pragma unroll
    for (int b = 0; b < 3; ++b) {
      const f32x4 gg = (acc[mb][b] - mr[0] * c4[b]) * mr[1], uu = (acc[mb][3 + b] - mr[0] * c4[3 + b]) * mr[1], hh = gg * uu;
      hv[b][0] = pk2(hh[0], hh[1]); hv[b][1] = pk2(hh[2], hh[3]);
    }
    zv[mb] = (acc[mb][6] - mr[0] * c4[6]) * mr[1];
    const u32x4 o = {hi ? hv[1][0] : hv[0][0], hi ? hv[1][1] : hv[0][1], hi ? hv[0][0] : hv[1][0], hi ? hv[0][1] : hv[1][1]};
    stc16(rws, v16, hb + mb * 114688, o);
    stc8(rws, v8, hb + mb * 114688 + 1024, hv[2]);
  }
#pragma unroll
  for (int mp = 0; mp < 2; ++mp) {
    float hh[4];
#pragma unroll
    for (int e = 0; e < 4; ++e) {
      const auto r = __builtin_amdgcn_permlane32_swap(__float_as_uint(zv[2 * mp][e]), __float_as_uint(zv[2 * mp + 1][e]), false, false);
      hh[e] = __uint_as_float(r[0]) * __uint_as_float(r[1]);
    }
    const u32x2 o = {pk2(hh[0], hh[1]), pk2(hh[2], hh[3])};
    stc8(rws, vz, hb + 2 * mp * 114688 + 1536, o);
  }
  BAR();
  c_signal<true>(P.ws, 64 * l + C_H + rt * 2 + (ct >> 4), lds, 5 * l + 3, 8);
  STAMP_END(4 + 5 * l);
}

constexpr int K4_NT = 28, K4_STAGE = 32768, K4_NS = 4;
DI void p4_load(const Params& P, int l, LAS char* lds, int j) {
  const int lane = fresh_lane();
  const int bid = fresh_s(blockIdx.x), xcd = bid & 7, loc = bid >> 3;
  const int kh = xcd & 1, rt = (xcd >> 1) * 4 + (loc & 3), ct = loc >> 2;
  const int rot = ROT_K4 ? (ct + 2 * (loc & 3)) % 7 : 0;
  const rsrc_t rA = mk_rsrc(P.ws, WS_BYTES), rW = mk_rsrc(P.wd, F_ * D_ * 2);
  const int r = lane >> 3, sl8 = lane & 7, f = ((r >> 1) & 1) | ((j & 1) << 1);
  const unsigned voffW = r * 2048 + ((sl8 ^ (2 * f)) * 16), voffL = lane * 16;
  auto stX = [&](int kt, int sl) {
    const unsigned sa = WS_H + (unsigned)((rt * 8 + 2 * j) * 114688 + kt * 2048);
#pragma unroll
    for (int u = 0; u < 4; ++u) dma16c(rA, lds + sl * K4_STAGE + (4 * j + u) * 1024, voffL, sa + (u >> 1) * 114688 + (u & 1) * 1024);
  };
  auto stW = [&](int kt, int sl) {
    const unsigned sw = (unsigned)((kt * 64 + j * 8) * 2048 + ct * 256);
#pragma unroll
    for (int u = 0; u < 4; ++u) dma16(rW, lds + sl * K4_STAGE + 16384 + (u >> 1) * 8192 + (j + 4 * (u & 1)) * 1024, voffW, sw + (u >> 1) * 128 + (u & 1) * 32 * 2048);
  };
  const int k0 = kh * K4_NT + rot, k1 = kh * K4_NT + (rot + 1) % 7, k2 = kh * K4_NT + (rot + 2) % 7;
  stW(k0, 0); stW(k1, 1); stW(k2, 2);
  l_wait(P.ws, 64 * l + C_H + (rt >> 1) * 2 + kh, 16, lds, 5 * l + 4, j);
  stX(k0, 0); stX(k1, 1); stX(k2, 2);
  int w0 = kh * K4_NT, wr_ = (rot + 3) % 7, sl = 3;
  auto next = [&]() { const int kt = w0 + wr_; stX(kt, sl); stW(kt, sl); wr_ = wr_ == 6 ? 0 : wr_ + 1; if (wr_ == rot) w0 += 7; sl = (sl + 1) & 3; };
  WAITV(8); BAR(); next();
  WAITV(12); BAR(); next();
  for (int t = 2; t < K4_NT - 3; ++t) { WAITV(16); BAR(); next(); }
  drain_bar<2, 8>();
  BAR();
}
DI void p4_comp(const Params& P, int l, LAS char* lds, int wid) {
  STAMP_DECL STAMP_AT(0);
  if (wid >= 4) {
#pragma unroll 1
    for (int i = 0; i < K4_NT + 1; ++i) BAR();
    return;
  }
  const int lane = fresh_lane();
  const int bid = fresh_s(blockIdx.x), xcd = bid & 7, loc = bid >> 3;
  const int kh = xcd & 1, rt = (xcd >> 1) * 4 + (loc & 3), ct = loc >> 2;
  const int wr = wid >> 1, wc = wid & 1;
  const int m = lane & 15, g = lane >> 4, q = (lane >> 2) & 3, p = lane & 3;
  const int row0 = rt * 128 + wr * 64 + kh * 32 + m;
  const unsigned off0 = (unsigned)(row0 * D_ + ct * 128 + wc * 64 + 8 * g) * 2;
  int wa[4];
  { const int f = ((q >> 1) & 1) | ((g & 1) << 1);
#pragma unroll
    for (int nb = 0; nb < 4; ++nb) wa[nb] = 16384 + wc * 8192 + (8 * g + q) * 128 + (((4 * (nb >> 1) + p) ^ (2 * f)) * 16) + (((nb & 1) ^ (p >> 1)) * 8); }
  const int xa0 = wr * 8192 + lane * 16 + kh * 4096, xa1 = wr * 8192 + lane * 16 + 4096 - kh * 4096;
  f32x4 acc[4][4];
#pragma unroll
  for (int a = 0; a < 4; ++a)
#pragma unroll
    for (int b = 0; b < 4; ++b) acc[a][b] = (f32x4){0.f, 0.f, 0.f, 0.f};
  bf16x8 xf[2][4], wf[2][4];
  auto fetch = [&](int s, int sl) {
    LAS char* b = lds + sl * K4_STAGE;
    xf[s][0] = ld128(b + xa0 + s * 1024); xf[s][1] = ld128(b + xa0 + s * 1024 + 2048);
    xf[s][2] = ld128(b + xa1 + s * 1024); xf[s][3] = ld128(b + xa1 + s * 1024 + 2048);
#pragma unroll
    for (int nb = 0; nb < 4; ++nb) wf[s][nb] = ldtr(b + wa[nb] + s * 4096, 512);
  };
  auto mma = [&](int s) {
#pragma unroll
    for (int nb = 0; nb < 4; ++nb)
#pragma unroll
      for (int mb = 0; mb < 4; ++mb) acc[mb][nb] = mfma(wf[s][nb], xf[s][mb], acc[mb][nb]);
  };
#define K4_MIX() do { _Pragma("unroll") for (int i_ = 0; i_ < 12; ++i_) { SGB(0x8, 1); SGB(0x100, 1); } SGB(0x8, 4); SB0(); } while (0)
  const brs_t rws = mk_brs(P.ws, WS_BYTES);
  BAR();
  STAMP_AT(1);
  fetch(0, 0);
  SB0();
  int sl = 0;
  for (int t = 0; t < K4_NT - 1; ++t) {
    fetch(1, sl); mma(0); K4_MIX();
    sl = (sl + 1) & (K4_NS - 1);
    WAITL0(); BAR();
    fetch(0, sl); mma(1); K4_MIX();
  }
  fetch(1, sl); mma(0); K4_MIX();
  mma(1);
  STAMP_AT(2);
  const int pair = rt * 8 + ct;
  u32x4 res[2][2];
#pragma unroll
  for (int mb = 0; mb < 2; ++mb) { res[mb][0] = ldc16(rws, off0, WS_X1 + mb * 32768); res[mb][1] = ldc16(rws, off0, WS_X1 + mb * 32768 + 64); }
  FENCE();
#pragma unroll
  for (int i = 0; i < 8; ++i) stc16(rws, lane * 16, WS_XCH + pair * 65536 + ((kh ^ 1) * 4 + wid) * 8192 + i * 1024, acc[2 + (i >> 2)][i & 3]);
  BAR();
#pragma unroll
  for (int mb = 0; mb < 2; ++mb) { res_unpack(acc[mb], res[mb], g >= 2, true); PIN(acc[mb][0]); PIN(acc[mb][1]); PIN(acc[mb][2]); PIN(acc[mb][3]); }
  WAITV(0);
  volatile LAS unsigned* relay = (volatile LAS unsigned*)(lds + L_SYNC + 48);
  { unsigned old = 0;
    if (lane == 0) old = __hip_atomic_fetch_add((LAS unsigned*)(lds + L_SYNC + 52), 1u, __ATOMIC_RELAXED, __HIP_MEMORY_SCOPE_WORKGROUP);
    old = __builtin_amdgcn_readfirstlane(old);
    if ((old & 3) == 3) {
      if (lane == 0) __hip_atomic_store(cnt_ptr(P.ws, C_FLAG + 256 * l + pair * 2 + kh), 1u, __ATOMIC_RELAXED, __HIP_MEMORY_SCOPE_AGENT);
      poll_ge(cnt_ptr(P.ws, C_FLAG + 256 * l + pair * 2 + (kh ^ 1)), 1);
      *relay = l + 1;
    } else { unsigned spins = 0; while ((unsigned)__builtin_amdgcn_readfirstlane(*relay) < (unsigned)(l + 1) && ++spins < (1u << 21)) __builtin_amdgcn_s_sleep(1); }
    FENCE(); }
  u32x4 in[8];
#pragma unroll
  for (int i = 0; i < 8; ++i) in[i] = ldc16(rws, lane * 16, WS_XCH + pair * 65536 + (kh * 4 + wid) * 8192 + i * 1024);
#pragma unroll
  for (int i = 0; i < 8; ++i) acc[i >> 2][i & 3] += __builtin_bit_cast(f32x4, in[i]);
  const brs_t rxo = mk_brs(P.out, S_ * D_ * 2);
#pragma unroll
  for (int mb = 0; mb < 2; ++mb)
    x_store(acc[mb], rxo, off0 + mb * 32768, rws, WS_ST + (unsigned)((2 * l + 2) & 3) * 262144u + ((ct * 2 + wc) * 2048 + row0 + mb * 16) * 8, m, g);
  if (l == 0) c_signal<true>(P.ws, 64 + C_X + rt, lds, 4, 4);
  STAMP_END(5 + 5 * l);
}

DI void init_sync(LAS char* lds) { if (threadIdx.x < 32) *(LAS unsigned*)(lds + L_SYNC + threadIdx.x * 4) = 0u; WAITL0(); }

__global__ __launch_bounds__(768, 3) void k_mega(Params P) {
  extern __shared__ __attribute__((aligned(16))) char smem[];
  LAS char* lds = (LAS char*)smem;
  const int wid = __builtin_amdgcn_readfirstlane(threadIdx.x >> 6);
  if (wid >= 8) {
    const int j = wid & 3;
#pragma unroll 1
    for (int l = 0; l < 2; ++l) { p1_load(P, fresh_s(l), lds, fresh_s(j)); p2_load<true>(P, fresh_s(l), lds, fresh_s(j)); p3_load(P, fresh_s(l), lds, fresh_s(j)); p4_load(P, fresh_s(l), lds, fresh_s(j)); }
  } else {
    init_sync(lds);
#pragma unroll 1
    for (int l = 0; l < 2; ++l) {
      p1_comp(P, fresh_s(l), lds, fresh_s(wid)); p2_comp(P, fresh_s(l), lds, fresh_s(wid));
      p3_comp(P, fresh_s(l), lds, fresh_s(wid)); p4_comp(P, fresh_s(l), lds, fresh_s(wid));
    }
  }
}

extern "C" void kernel_launch(void* const* d_in, const int* in_sizes, int n_in, void* d_out, int out_size, void* d_ws, size_t ws_size, hipStream_t stream) {
  Params P;
  P.x0 = (const u16*)d_in[0]; P.wqkv = (const u16*)d_in[1]; P.wo = (const u16*)d_in[2];
  P.wg = (const u16*)d_in[4]; P.wu = (const u16*)d_in[5]; P.wd = (const u16*)d_in[6];
  P.out = (u16*)d_out; P.ws = (char*)d_ws;
  k0_prep<<<K0_GRID, 256, 0, stream>>>(P);
  (void)hipFuncSetAttribute((const void*)k_mega, hipFuncAttributeMaxDynamicSharedMemorySize, L_BYTES);
  k_mega<<<256, 768, L_BYTES, stream>>>(P);
}
```

```cpp
#include <hip/hip_runtime.h>
#include <hip/hip_bf16.h>
#include <stdint.h>

#define LAS __attribute__((address_space(3)))
#define DI __device__ __forceinline__
typedef unsigned short u16;
typedef __bf16 bf16x8 __attribute__((ext_vector_type(8)));
typedef __bf16 bf16x2 __attribute__((ext_vector_type(2)));
typedef short s16x4 __attribute__((ext_vector_type(4)));
typedef float f32x2 __attribute__((ext_vector_type(2)));
typedef float f32x4 __attribute__((ext_vector_type(4)));
typedef unsigned u32x2 __attribute__((ext_vector_type(2)));
typedef unsigned u32x4 __attribute__((ext_vector_type(4)));
typedef u32x4 rsrc_t;
typedef __amdgpu_buffer_rsrc_t brs_t;

constexpr int S_ = 2048, D_ = 1024, F_ = 3584;
#define MEGA 1
#define XCH_BF16 0
#define ROT_K4B 1
#define K0_GRID 768
#define K0_NT 1
#define PF_GU 0
#define PF_ON 1
#define POLL_SLEEP 1
#define POLL_PIPE 0
#define ROT_K1 0
#define ROT_K3 0
#define ROT_K2 0
#define ROT_K4 1
constexpr unsigned OOB_V = 0x80000000u;
constexpr unsigned WS_PQ = 0, WS_KVP = 4u << 20, WS_X1 = 8u << 20, WS_H = 12u << 20, WS_XCH = 27u << 20, WS_ST = 35u << 20, WS_CS = 36u << 20, WS_CNT = (36u << 20) + (1u << 19),
                   WS_BYTES = 38u << 20;
#define NREP 8
constexpr int C_X = 0, C_KV = 16, C_O = 32, C_X1 = 40, C_H = 48, C_FLAG = 128 * NREP, C_N = C_FLAG + 512;
constexpr int L_STATS = 147456, L_CS = L_STATS + 2048, L_KV = L_CS + 1024, L_SYNC = L_KV + 4224, L_BYTES = L_SYNC + 128;

struct Params { const u16* x0; const u16* wqkv; const u16* wo; const u16* wg; const u16* wu; const u16* wd; u16* out; char* ws; };

DI rsrc_t mk_rsrc(const void* p, unsigned bytes) {
  const unsigned long long a = (unsigned long long)p;
  rsrc_t r = {(unsigned)a, (unsigned)(a >> 32) & 0xffffu, bytes, 0x00020000u};
  return r;
}
DI brs_t mk_brs(const void* p, unsigned bytes) { return __builtin_amdgcn_make_buffer_rsrc((void*)p, 0, bytes, 0x00020000); }
DI void dma16(rsrc_t r, LAS char* dst, unsigned voff, unsigned soff) {
  asm volatile("s_mov_b32 m0, %0\n\ts_nop 0\n\tbuffer_load_dwordx4 %1, %2, %3 offen lds" :: "s"((unsigned)(size_t)dst), "v"(voff), "s"(r), "s"(soff) : "memory");
}
DI void dma16c(rsrc_t r, LAS char* dst, unsigned voff, unsigned soff) {
  asm volatile("s_mov_b32 m0, %0\n\ts_nop 0\n\tbuffer_load_dwordx4 %1, %2, %3 offen sc1 lds" :: "s"((unsigned)(size_t)dst), "v"(voff), "s"(r), "s"(soff) : "memory");
}
DI u32x4 ldc16(brs_t r, unsigned voff, unsigned soff) { return __builtin_amdgcn_raw_buffer_load_b128(r, voff, soff, 16); }
DI void stc16(brs_t r, unsigned voff, unsigned soff, u32x4 v) { __builtin_amdgcn_raw_buffer_store_b128(v, r, voff, soff, 16); }
DI void stc16(brs_t r, unsigned voff, unsigned soff, f32x4 v) { stc16(r, voff, soff, __builtin_bit_cast(u32x4, v)); }
DI void stc8(brs_t r, unsigned voff, unsigned soff, u32x2 v) { __builtin_amdgcn_raw_buffer_store_b64(v, r, voff, soff, 16); }
DI f32x4 mfma(bf16x8 a, bf16x8 b, f32x4 c) { return __builtin_amdgcn_mfma_f32_16x16x32_bf16(a, b, c, 0, 0, 0); }
DI bf16x8 ld128(LAS char* p) { return *(LAS bf16x8*)p; }
DI bf16x8 ldtr(LAS char* p, int off2) {
  s16x4 a = __builtin_amdgcn_ds_read_tr16_b64_v4i16((LAS s16x4*)p);
  s16x4 b = __builtin_amdgcn_ds_read_tr16_b64_v4i16((LAS s16x4*)(p + off2));
  return __builtin_bit_cast(bf16x8, __builtin_shufflevector(a, b, 0, 1, 2, 3, 4, 5, 6, 7));
}
DI unsigned pk2(float a, float b) { f32x2 v = {a, b}; return __builtin_bit_cast(unsigned, __builtin_convertvector(v, bf16x2)); }
DI float bflo(unsigned u) { return __uint_as_float(u << 16); }
DI float bfhi(unsigned u) { return __uint_as_float(u & 0xffff0000u); }
DI float phi_ln(float u, float rs, float rs2) {
  float e; asm("v_exp_f32 %0, %1 clamp" : "=v"(e) : "v"(u * rs2));
  return fmaxf(__builtin_fmaf(u, rs, 1e-6f), 1e-6f) + e;
}
DI float xsum(float s) {
  const auto a = __builtin_amdgcn_permlane16_swap(__float_as_uint(s), __float_as_uint(s), false, false);
  s = __uint_as_float(a[0]) + __uint_as_float(a[1]);
  const auto b = __builtin_amdgcn_permlane32_swap(__float_as_uint(s), __float_as_uint(s), false, false);
  return __uint_as_float(b[0]) + __uint_as_float(b[1]);
}
#define SGB(mask, n) __builtin_amdgcn_sched_group_barrier(mask, n, 0)
#define SB0() __builtin_amdgcn_sched_barrier(0)
#define PIN(x) asm volatile("" : "+v"(x))
#define FENCE() asm volatile("" ::: "memory")
#define BAR() do { FENCE(); __builtin_amdgcn_s_barrier(); FENCE(); __builtin_amdgcn_sched_barrier(0); } while (0)
#define WAITV(n) asm volatile("s_waitcnt vmcnt(%0)" :: "i"(n) : "memory")
#define WAITL0() do { FENCE(); __builtin_amdgcn_s_waitcnt(0xc07f); FENCE(); } while (0)
DI int fresh_s(int x) { asm volatile("" : "+s"(x)); return x; }
DI int fresh_lane() { int t; asm volatile("v_mbcnt_lo_u32_b32 %0, -1, 0\n\tv_mbcnt_hi_u32_b32 %0, -1, %0" : "=v"(t)); return t; }

#define STAMP_DECL
#define STAMP_AT(i)
#define STAMP_END(sl_)

template <int N> DI void pf_lines(const void* p) {
#pragma unroll
  for (int i = 0; i < 2 * N; ++i) asm volatile("s_load_dword s100, %0, %1" :: "s"(p), "i"(i * 64) : "s100");
}
DI unsigned* cnt_ptr(char* ws, int idx) { return (unsigned*)(ws + WS_CNT + idx * 128); }
DI unsigned* rep_ptr(char* ws, int idx) { return cnt_ptr(ws, idx * NREP + ((blockIdx.x >> 3) + (blockIdx.x & 7) * 5) % NREP); }
DI void poll_ge(unsigned* c, unsigned target) {
  unsigned spins = 0;
  while ((unsigned)__builtin_amdgcn_readfirstlane(__hip_atomic_load(c, __ATOMIC_RELAXED, __HIP_MEMORY_SCOPE_AGENT)) < target && ++spins < (1u << 18)) __builtin_amdgcn_s_sleep(POLL_SLEEP);
  FENCE();
}
DI void l_wait(char* ws, int idx, unsigned target, LAS char* lds, unsigned epoch, int j) {
  if (!MEGA) return;
  volatile LAS unsigned* relay = (volatile LAS unsigned*)(lds + L_SYNC);
  if (j == 0) { poll_ge(rep_ptr(ws, idx), target); *relay = epoch; }
  else { unsigned spins = 0; while ((unsigned)__builtin_amdgcn_readfirstlane(*relay) < epoch && ++spins < (1u << 21)) __builtin_amdgcn_s_sleep(1); }
  FENCE();
}
template <bool REP> DI void c_signal(char* ws, int idx, LAS char* lds, int arr, unsigned nw) {
  if (!MEGA) return;
  WAITV(0);
  const int lane = fresh_lane();
  unsigned old = 0;
  if (lane == 0) old = __hip_atomic_fetch_add((LAS unsigned*)(lds + L_SYNC + 4 + arr * 4), 1u, __ATOMIC_RELAXED, __HIP_MEMORY_SCOPE_WORKGROUP);
  if ((unsigned)__builtin_amdgcn_readfirstlane(old) == nw - 1 && lane < (REP ? NREP : 1))
    __hip_atomic_fetch_add(cnt_ptr(ws, REP ? idx * NREP + lane : idx), 1u, __ATOMIC_RELAXED, __HIP_MEMORY_SCOPE_AGENT);
}

template <int NT, bool PAIR> DI f32x2 combine_stats(const u32x2 (&u)[NT]) {
  float s = 0.f;
#pragma unroll
  for (int i = 0; i < NT; ++i) s += __uint_as_float(u[i][0]);
  if (PAIR) s += __shfl_xor(s, 1);
  const float mean = s * (1.f / 1024.f);
  float q = 0.f;
#pragma unroll
  for (int i = 0; i < NT; ++i) { const float d = __uint_as_float(u[i][0]) * (1.f / 64.f) - mean; q += __uint_as_float(u[i][1]) + 64.f * d * d; }
  if (PAIR) q += __shfl_xor(q, 1);
  f32x2 r; r[0] = mean; r[1] = 1.0f / sqrtf(q * (1.f / 1024.f) + 1e-5f);
  return r;
}
DI u32x4 ones_bf16() { unsigned o = 0x3f803f80u; asm volatile("" : "+v"(o)); const u32x4 r = {o, o, o, o}; return r; }
DI void res_unpack(f32x4 (&a)[4], const u32x4 (&res)[2], bool hi, bool add) {
#pragma unroll
  for (int pr = 0; pr < 2; ++pr) {
    const unsigned u0 = hi ? res[pr][2] : res[pr][0], u1 = hi ? res[pr][3] : res[pr][1], u2 = hi ? res[pr][0] : res[pr][2], u3 = hi ? res[pr][1] : res[pr][3];
    const f32x4 x = {bflo(u0), bfhi(u0), bflo(u1), bfhi(u1)}, y = {bflo(u2), bfhi(u2), bflo(u3), bfhi(u3)};
    if (add) { a[2 * pr] += x; a[2 * pr + 1] += y; } else { a[2 * pr] = x; a[2 * pr + 1] = y; }
  }
}
DI void x_store(const f32x4 (&a)[4], brs_t rxo, unsigned xoff, brs_t rws, unsigned stoff, int m, int g) {
  const bool hi = g >= 2;
  const u32x4 ones4 = ones_bf16();
  f32x4 sm = {0.f, 0.f, 0.f, 0.f}, sq = {0.f, 0.f, 0.f, 0.f};
#pragma unroll
  for (int pr = 0; pr < 2; ++pr) {
    const unsigned p0 = pk2(a[2 * pr][0], a[2 * pr][1]), p1 = pk2(a[2 * pr][2], a[2 * pr][3]), p2 = pk2(a[2 * pr + 1][0], a[2 * pr + 1][1]), p3 = pk2(a[2 * pr + 1][2], a[2 * pr + 1][3]);
    const u32x4 o = {hi ? p2 : p0, hi ? p3 : p1, hi ? p0 : p2, hi ? p1 : p3};
    stc16(rxo, xoff, pr * 64, o);
    const bf16x8 f = __builtin_bit_cast(bf16x8, o);
    sm = mfma(__builtin_bit_cast(bf16x8, ones4), f, sm);
    sq = mfma(f, f, sq);
  }
  const float dq = (m & 2) ? ((m & 1) ? sq[3] : sq[2]) : ((m & 1) ? sq[1] : sq[0]);
  if (g == (m >> 2)) { const u32x2 r = {__float_as_uint(sm[0]), __float_as_uint(fmaxf(dq - sm[0] * sm[0] * (1.f / 64.f), 0.f))}; stc8(rws, stoff, 0, r); }
}
template <int N, int NI> DI void drain_bar() { WAITV(N * NI); BAR(); if constexpr (N > 0) drain_bar<N - 1, NI>(); }

__global__ __launch_bounds__(256) void k0_prep(Params P) {
  __shared__ float red[32][65];
  STAMP_DECL STAMP_AT(0); STAMP_AT(1); STAMP_AT(2);
  const int b = blockIdx.x, t = threadIdx.x;
  float* stats = (float*)(P.ws + WS_ST); float* cs = (float*)(P.ws + WS_CS);
  if (b * 256 + t < C_N) *cnt_ptr(P.ws, b * 256 + t) = 0u;
  if (b < 128) {
    const int row = b * 16 + (t >> 4), tile = t & 15;
    const u32x4* p = (const u32x4*)(P.x0 + (size_t)row * D_ + tile * 64);
    u32x4 v[8];
#pragma unroll
    for (int i = 0; i < 8; ++i) v[i] = p[i];
    float s = 0.f;
#pragma unroll
    for (int i = 0; i < 8; ++i)
#pragma unroll
      for (int j = 0; j < 4; ++j) s += bflo(v[i][j]) + bfhi(v[i][j]);
    const float m = s * (1.f / 64.f); float q = 0.f;
#pragma unroll
    for (int i = 0; i < 8; ++i)
#pragma unroll
      for (int j = 0; j < 4; ++j) { const float a = bflo(v[i][j]) - m, c = bfhi(v[i][j]) - m; q += a * a + c * c; }
    f32x2 r = {s, q};
    *(f32x2*)(stats + ((size_t)tile * 2048 + row) * 2) = r;
  } else if (b < K0_GRID) {
    const int pn = (b - 128) >> 2, kq = (b - 128) & 3;
    const u16* w; int ld, col, outoff;
    if (pn < 48) { w = P.wqkv; ld = 3072; col = pn * 64; outoff = col; }
    else if (pn < 104) { w = P.wg; ld = F_; col = (pn - 48) * 64; outoff = 3072 + col; }
    else { w = P.wu; ld = F_; col = (pn - 104) * 64; outoff = 3072 + F_ + col; }
    const int c8 = t & 7, r0 = t >> 3;
    const u32x4* src = (const u32x4*)(w + (size_t)(kq * 256 + r0) * ld + col + c8 * 8);
    u32x4 v[8];
    if (K0_NT == 2 || (K0_NT && pn >= 48)) {
#pragma unroll
      for (int i = 0; i < 8; ++i) v[i] = __builtin_nontemporal_load(src + (size_t)i * 4 * ld);
    } else {
#pragma unroll
      for (int i = 0; i < 8; ++i) v[i] = src[(size_t)i * 4 * ld];
    }
    float a[8] = {0, 0, 0, 0, 0, 0, 0, 0};
#pragma unroll
    for (int i = 0; i < 8; ++i)
#pragma unroll
      for (int j = 0; j < 4; ++j) { a[2 * j] += bflo(v[i][j]); a[2 * j + 1] += bfhi(v[i][j]); }
#pragma unroll
    for (int j = 0; j < 8; ++j) red[r0][c8 * 8 + j] = a[j];
    __syncthreads();
    if (t < 64) { float s = 0.f;
#pragma unroll
      for (int r = 0; r < 32; ++r) s += red[r][t];
      cs[(outoff + t) * 4 + kq] = s; }
  }
#define STAMP_WID __builtin_amdgcn_readfirstlane(threadIdx.x >> 6)
  STAMP_END(0);
#undef STAMP_WID
#define STAMP_WID wid
}

#define ALOAD8C(dst, rs, voff, soff) asm volatile("buffer_load_dwordx2 %0, %1, %2, %3 offen sc1" : "=v"(dst) : "v"(voff), "s"(rs), "s"(soff) : "memory")
#define ALOAD16(dst, rs, voff, soff) asm volatile("buffer_load_dwordx4 %0, %1, %2, %3 offen" : "=v"(dst) : "v"(voff), "s"(rs), "s"(soff) : "memory")

constexpr int K1_STAGE = 40960, K1_RED = 49152;
DI void p1_load(const Params& P, int l, LAS char* lds, int j) {
  const int lane = fresh_lane(), tid = j * 64 + lane;
  const int bid = fresh_s(blockIdx.x), xcd = bid & 7, loc = bid >> 3;
  const int rt = (xcd & 1) * 8 + (loc & 7), hp = (xcd >> 1) * 4 + (loc >> 3);
  const int rot = ROT_K1 ? ((loc & 7) + 2 * (loc >> 3)) & 7 : 0;
  const rsrc_t rX = mk_rsrc(l ? P.out : P.x0, S_ * D_ * 2), rW = mk_rsrc(P.wqkv, D_ * 3072 * 2), rS = mk_rsrc(P.ws, WS_BYTES);
  const int r = lane >> 3, sl8 = lane & 7, f = ((r >> 1) & 1) | ((j & 1) << 1);
  const unsigned voffX = r * 2048 + ((sl8 ^ (r & 6)) * 16), voffW = r * 6144 + ((sl8 ^ (2 * f)) * 16);
  auto stX = [&](int t, int st) {
    const unsigned sx = (unsigned)((rt * 128 + j * 32) * 2048 + ((t & 8) | ((t + rot) & 7)) * 128);
#pragma unroll
    for (int u = 0; u < 4; ++u) dma16c(rX, lds + st * K1_STAGE + (4 * j + u) * 1024, voffX, sx + u * 16384);
  };
  auto stW = [&](int t, int st) {
    const unsigned sw = (unsigned)((((t & 8) | ((t + rot) & 7)) * 64 + j * 8) * 6144 + hp * 128);
#pragma unroll
    for (int s = 0; s < 2; ++s)
#pragma unroll
      for (int pnl = 0; pnl < 3; ++pnl) dma16(rW, lds + st * K1_STAGE + 16384 + pnl * 8192 + (4 * s + j) * 1024, voffW, sw + pnl * 2048 + s * 32 * 6144);
  };
  stW(0, 0); stW(1, 1);
  if (l) l_wait(P.ws, 64 + C_X + rt, 16, lds, 5, j);
  stX(0, 0); stX(1, 1);
  u32x2 sv[8]; f32x4 csv;
  { const int t = tid;
    const unsigned so = WS_ST + (unsigned)(2 * l) * 262144u + rt * 1024, vo = (t & 1) * 131072 + (t >> 1) * 8;
#pragma unroll
    for (int i = 0; i < 8; ++i) ALOAD8C(sv[i], rS, vo, so + i * 16384);
    const int ci = t < 192 ? t : 0;
    ALOAD16(csv, rS, ((ci >> 6) * 1024 + (ci & 63)) * 16, WS_CS + hp * 1024); }
  WAITV(13); BAR();
  stX(2, 2); stW(2, 2);
  WAITV(19); BAR();
  stX(3, 0); stW(3, 0);
  WAITV(10); BAR();
  stX(4, 1); stW(4, 1);
  { const int t = tid;
#pragma unroll
    for (int i = 0; i < 8; ++i) PIN(sv[i]);
    PIN(csv);
    const f32x2 mr = combine_stats<8, true>(sv);
    if (!(t & 1)) *(LAS f32x2*)(lds + L_STATS + (t >> 1) * 8) = mr;
    if (t < 192) *(LAS float*)(lds + L_CS + t * 4) = (csv[0] + csv[1]) + (csv[2] + csv[3]);
    WAITL0(); }
  WAITV(10); BAR();
  int st = 2;
  for (int t = 3; t < 14; ++t) {
    stX(t + 2, st); stW(t + 2, st);
    if (l == 0 && t < 11) pf_lines<2>((const char*)P.wo + ((bid * 4 + j) * 16 + (t - 3) * 2) * 128);
    if (PF_GU && l == 0 && t < 13) pf_lines<PF_GU>((const char*)((j & 1) ? P.wu : P.wg) + ((bid * 2 + (j >> 1)) * 112 + (t - 3) * PF_GU) * 128);
    WAITV(10); BAR();
    st = st == 2 ? 0 : st + 1;
  }
  WAITV(0); BAR();
  BAR();
  BAR(); BAR();
}
DI void p1_comp(const Params& P, int l, LAS char* lds, int wid) {
  STAMP_DECL STAMP_AT(0);
  const int bid = fresh_s(blockIdx.x), xcd = bid & 7, loc = bid >> 3;
  const int rt = (xcd & 1) * 8 + (loc & 7), hp = (xcd >> 1) * 4 + (loc >> 3);
  if (wid >= 4) {
#pragma unroll 1
    for (int i = 0; i < 19; ++i) BAR();
    STAMP_AT(0);
    volatile LAS unsigned* relay = (volatile LAS unsigned*)(lds + L_SYNC + 56);
    if (wid == 4) { poll_ge(rep_ptr(P.ws, 64 * l + C_KV + hp), 16); *relay = l + 1; }
    else { unsigned spins = 0; while ((unsigned)__builtin_amdgcn_readfirstlane(*relay) < (unsigned)(l + 1) && ++spins < (1u << 21)) __builtin_amdgcn_s_sleep(1); }
    FENCE();
    STAMP_AT(1);
    const int lane = fresh_lane(), hw = (wid >> 1) & 1, wr = wid & 1;
    const brs_t rws = mk_brs(P.ws, WS_BYTES);
    const unsigned src = WS_KVP + (2 * hp + hw) * 2176;
    u32x4 v[16];
#pragma unroll
    for (int i = 0; i < 16; ++i) v[i] = ldc16(rws, lane * 16, src + i * 32 * 2176 + wr * 1024);
    const u32x2 zv = __builtin_amdgcn_raw_buffer_load_b64(rws, (lane >> 2) * (32 * 2176) + (lane & 3) * 8, src + 2048 + wr * 32, 16);
    float s[8] = {0.f, 0.f, 0.f, 0.f, 0.f, 0.f, 0.f, 0.f};
#pragma unroll
    for (int i = 0; i < 16; ++i)
#pragma unroll
      for (int e = 0; e < 4; ++e) { s[2 * e] += bflo(v[i][e]); s[2 * e + 1] += bfhi(v[i][e]); }
#pragma unroll
    for (int b = 0; b < 2; ++b) {
      const int e = 16 * b + (lane & 15), i16 = (e >> 3) * 4 + (e & 3), bp = (e >> 2) & 1;
      const u32x2 o = {pk2(s[4 * b], s[4 * b + 1]), pk2(s[4 * b + 2], s[4 * b + 3])};
      *(LAS u32x2*)(lds + L_KV + hw * 2112 + bp * 1024 + ((lane >> 4) * 16 + i16) * 16 + 8 * wr) = o;
    }
    float z[4] = {bflo(zv[0]), bfhi(zv[0]), bflo(zv[1]), bfhi(zv[1])};
#pragma unroll
    for (int e = 0; e < 4; ++e) {
      z[e] += __uint_as_float(__builtin_amdgcn_update_dpp(0u, __float_as_uint(z[e]), 0x124, 0xf, 0xf, false));
      z[e] += __uint_as_float(__builtin_amdgcn_update_dpp(0u, __float_as_uint(z[e]), 0x128, 0xf, 0xf, false));
      z[e] = xsum(z[e]);
    }
    if (lane < 4) { const u32x2 oz = {pk2(z[0], z[1]), pk2(z[2], z[3])}; *(LAS u32x2*)(lds + L_KV + hw * 2112 + 2048 + lane * 16 + 8 * wr) = oz; }
    WAITL0();
    BAR();
    return;
  }
  const int wr = wid >> 1, wc = wid & 1;
  f32x4 acc[3][4][2];
  {
    const int lane = fresh_lane(), m = lane & 15, g = lane >> 4, q = (lane >> 2) & 3, p = lane & 3;
    int xa0 = (wr * 64 + m) * 128 + ((g ^ (m & 6)) * 16), xa1 = xa0 ^ 64;
    int wa0 = 16384 + (8 * g + q) * 128 + (((4 * wc + (p >> 1)) ^ (2 * (((q >> 1) & 1) | ((g & 1) << 1)))) * 16) + (p & 1) * 8, wa1 = wa0 ^ 32;
#pragma unroll
    for (int a = 0; a < 3; ++a)
#pragma unroll
      for (int b = 0; b < 4; ++b)
#pragma unroll
        for (int c = 0; c < 2; ++c) acc[a][b][c] = (f32x4){0.f, 0.f, 0.f, 0.f};
    bf16x8 xf[4], wf[2][3][2];
    auto step = [&](int cur, bool bar) {
      constexpr bool more = true;
      LAS char* xp = lds + (cur ? xa0 : xa1); LAS char* w0 = lds + wa0 + (cur ? 0 : 4096); LAS char* w1 = lds + wa1 + (cur ? 0 : 4096);
#pragma unroll
      for (int mb = 0; mb < 4; ++mb) {
        const bf16x8 x = xf[mb];
        if (more && mb == 1) { wf[cur ^ 1][0][0] = ldtr(w0, 512); xf[0] = ld128(xp); wf[cur ^ 1][1][0] = ldtr(w0 + 8192, 512); }
        if (more && mb == 2) { wf[cur ^ 1][2][0] = ldtr(w0 + 16384, 512); xf[1] = ld128(xp + 2048); wf[cur ^ 1][0][1] = ldtr(w1, 512); }
        if (more && mb == 3) { wf[cur ^ 1][1][1] = ldtr(w1 + 8192, 512); xf[2] = ld128(xp + 4096); wf[cur ^ 1][2][1] = ldtr(w1 + 16384, 512); }
#pragma unroll
        for (int nb = 0; nb < 2; ++nb) {
          acc[0][mb][nb] = mfma(wf[cur][0][nb], x, acc[0][mb][nb]);
          acc[1][mb][nb] = mfma(x, wf[cur][1][nb], acc[1][mb][nb]);
          acc[2][mb][nb] = mfma(x, wf[cur][2][nb], acc[2][mb][nb]);
        }
        if (mb == 0 && cur) { SB0(); if (bar) { WAITL0(); BAR(); } }
        if (more && mb == 3) xf[3] = ld128(xp + 6144);
        if (more && mb > 0) { _Pragma("unroll") for (int i_ = 0; i_ < 5; ++i_) { SGB(0x8, 1); SGB(0x100, 1); } SGB(0x8, 1); SGB(0x100, 1); }
        SB0();
      }
    };
    BAR();
    STAMP_AT(1);
#pragma unroll
    for (int mb = 0; mb < 4; ++mb) xf[mb] = ld128(lds + xa0 + mb * 2048);
#pragma unroll
    for (int pnl = 0; pnl < 3; ++pnl) { wf[0][pnl][0] = ldtr(lds + wa0 + pnl * 8192, 512); wf[0][pnl][1] = ldtr(lds + wa1 + pnl * 8192, 512); }
    SB0();
    int st = 0;
    for (int t = 0; t < 16; ++t) {
      step(0, false);
      const int d = st == 2 ? -2 * K1_STAGE : K1_STAGE; st = st == 2 ? 0 : st + 1;
      xa0 += d; xa1 += d; wa0 += d; wa1 += d; PIN(xa0); PIN(xa1); PIN(wa0); PIN(wa1);
      step(1, true);
    }
  }
  STAMP_AT(2);
  const int lane = fresh_lane(), m = lane & 15, g = lane >> 4;
  const brs_t rws = mk_brs(P.ws, WS_BYTES);
  const int head = 2 * hp + wc, trow = wr * 64;
  const LAS f32x2* stt = (const LAS f32x2*)(lds + L_STATS);
  const LAS float* csl = (const LAS float*)(lds + L_CS);
  const u32x4 ones4 = ones_bf16();
  f32x4 kvr[6];
#pragma unroll
  for (int i = 0; i < 6; ++i) kvr[i] = (f32x4){0.f, 0.f, 0.f, 0.f};
  float ck[2], cv[2];
#pragma unroll
  for (int nb = 0; nb < 2; ++nb) { ck[nb] = csl[64 + wc * 32 + nb * 16 + m]; cv[nb] = csl[128 + wc * 32 + nb * 16 + m]; }
#pragma unroll
  for (int mp = 0; mp < 2; ++mp) {
    u32x4 pkf[2], vf[2];
#pragma unroll
    for (int hb = 0; hb < 2; ++hb) {
      const int mb = 2 * mp + hb;
      const f32x4 a0 = *(const LAS f32x4*)(stt + trow + mb * 16 + 4 * g), a1 = *(const LAS f32x4*)(stt + trow + mb * 16 + 4 * g + 2);
      const float mean[4] = {a0[0], a0[2], a1[0], a1[2]}, rstd[4] = {a0[1], a0[3], a1[1], a1[3]};
#pragma unroll
      for (int nb = 0; nb < 2; ++nb) {
        float kk[4], vv[4];
#pragma unroll
        for (int e = 0; e < 4; ++e) { kk[e] = phi_ln(acc[1][mb][nb][e] - mean[e] * ck[nb], rstd[e], rstd[e] * 1.44269504f); vv[e] = rstd[e] * (acc[2][mb][nb][e] - mean[e] * cv[nb]); }
        pkf[nb][2 * hb] = pk2(kk[0], kk[1]); pkf[nb][2 * hb + 1] = pk2(kk[2], kk[3]);
        vf[nb][2 * hb] = pk2(vv[0], vv[1]); vf[nb][2 * hb + 1] = pk2(vv[2], vv[3]);
      }
      SB0();
    }
#pragma unroll
    for (int a = 0; a < 2; ++a) {
#pragma unroll
      for (int b = 0; b < 2; ++b) kvr[a * 3 + b] = mfma(__builtin_bit_cast(bf16x8, pkf[a]), __builtin_bit_cast(bf16x8, vf[b]), kvr[a * 3 + b]);
      kvr[a * 3 + 2] = mfma(__builtin_bit_cast(bf16x8, pkf[a]), __builtin_bit_cast(bf16x8, ones4), kvr[a * 3 + 2]);
    }
  }
  f32x4 own[3];
#pragma unroll
  for (int i = 0; i < 3; ++i) {
    f32x4 snd;
#pragma unroll
    for (int e = 0; e < 4; ++e) { snd[e] = wr ? kvr[i][e] : kvr[3 + i][e]; own[i][e] = wr ? kvr[3 + i][e] : kvr[i][e]; }
    *(LAS f32x4*)(lds + K1_RED + wid * 3072 + i * 1024 + lane * 16) = snd;
  }
  WAITL0();
  BAR();
#pragma unroll
  for (int i = 0; i < 3; ++i) own[i] += *(LAS f32x4*)(lds + K1_RED + (wid ^ 2) * 3072 + i * 1024 + lane * 16);
  { const unsigned dst = WS_KVP + (rt * 32 + head) * 2176;
    const u32x4 o = {pk2(own[0][0], own[0][1]), pk2(own[0][2], own[0][3]), pk2(own[1][0], own[1][1]), pk2(own[1][2], own[1][3])};
    stc16(rws, lane * 16, dst + wr * 1024, o);
    const u32x2 oz = {pk2(own[2][0], own[2][1]), pk2(own[2][2], own[2][3])};
    if (m == 0) stc8(rws, g * 8, dst + 2048 + wr * 32, oz); }
  BAR();
  c_signal<true>(P.ws, 64 * l + C_KV + hp, lds, 5 * l + 0, 4);
  f32x4 c4[2];
#pragma unroll
  for (int nb = 0; nb < 2; ++nb) c4[nb] = *(const LAS f32x4*)(csl + wc * 32 + nb * 16 + 4 * g);
  u32x4 pq[4];
#pragma unroll
  for (int mb = 0; mb < 4; ++mb) {
    const f32x2 mr = stt[trow + mb * 16 + m];
    const float rs2 = mr[1] * 1.44269504f;
#pragma unroll
    for (int nb = 0; nb < 2; ++nb) {
      f32x4 v;
#pragma unroll
      for (int e = 0; e < 4; ++e) v[e] = phi_ln(acc[0][mb][nb][e] - mr[0] * c4[nb][e], mr[1], rs2);
      pq[mb][2 * nb] = pk2(v[0], v[1]); pq[mb][2 * nb + 1] = pk2(v[2], v[3]);
    }
    PIN(pq[mb]);
  }
  STAMP_END(1 + 5 * l);
  STAMP_AT(0);
  BAR();
  STAMP_AT(1); STAMP_AT(2);
  { LAS char* kb = lds + L_KV + wc * 2112;
    const bf16x8 k0 = ld128(kb + lane * 16), k1 = ld128(kb + 1024 + lane * 16), zf = ld128(kb + 2048 + g * 16);
    const f32x4 z4 = {0.f, 0.f, 0.f, 0.f};
#pragma unroll
    for (int mb = 0; mb < 4; ++mb) {
      const bf16x8 x = __builtin_bit_cast(bf16x8, pq[mb]);
      const f32x4 dn = mfma(zf, x, z4), d0 = mfma(k0, x, z4), d1 = mfma(k1, x, z4);
      const float rr = __builtin_amdgcn_rcpf(dn[0] + 1e-6f);
      const u32x4 o = {pk2(d0[0] * rr, d0[1] * rr), pk2(d0[2] * rr, d0[3] * rr), pk2(d1[0] * rr, d1[1] * rr), pk2(d1[2] * rr, d1[3] * rr)};
      stc16(rws, lane * 16, WS_PQ + ((rt * 8 + wr * 4 + mb) * 32 + head) * 1024, o);
    } }
  c_signal<true>(P.ws, 64 * l + C_O + (rt >> 1), lds, 5 * l + 1, 4);
  STAMP_END(2 + 5 * l);
}

constexpr int K2_NT = 16, K2_STAGE = 24576, K2_NS = 6;
template <bool P1B> DI void p2_load(const Params& P, int l, LAS char* lds, int j) {
  const int lane = fresh_lane();
  const int bid = fresh_s(blockIdx.x), xcd = bid & 7, loc = bid >> 3;
  const int rt = (xcd & 3) * 4 + (loc & 3), ct = (xcd >> 2) * 8 + (loc >> 2);
  const int rot = ROT_K2 ? ((loc >> 2) + 2 * (loc & 3)) & 7 : 0;
  const rsrc_t rA = mk_rsrc(P.ws, WS_BYTES), rW = mk_rsrc(P.wo, D_ * D_ * 2);
  const int r = lane >> 3, sl8 = lane & 7, f = ((r >> 1) & 1) | ((j & 1) << 1);
  const unsigned voffW = r * 2048 + ((sl8 ^ (2 * f)) * 16), voffL = lane * 16;
  auto stX = [&](int t, int sl) {
    const unsigned sa = WS_PQ + (unsigned)((rt * 8 + 2 * j) * 32768 + ((t & ~7) | ((t + rot) & 7)) * 2048);
#pragma unroll
    for (int u = 0; u < 4; ++u) dma16c(rA, lds + sl * K2_STAGE + (4 * j + u) * 1024, voffL, sa + (u >> 1) * 32768 + (u & 1) * 1024);
  };
  auto stW = [&](int t, int sl) {
    const unsigned sw = (unsigned)((((t & ~7) | ((t + rot) & 7)) * 64 + j * 8) * 2048 + ct * 128);
#pragma unroll
    for (int u = 0; u < 2; ++u) dma16(rW, lds + sl * K2_STAGE + 16384 + (j + 4 * u) * 1024, voffW, sw + u * 32 * 2048);
  };
#pragma unroll
  for (int t = 0; t < 5; ++t) stW(t, t);
  if (P1B) BAR();
  l_wait(P.ws, 64 * l + C_O + (rt >> 1), 32, lds, 5 * l + 2, j);
#pragma unroll
  for (int t = 0; t < 5; ++t) stX(t, t);
  WAITV(16); BAR(); stX(5, 5); stW(5, 5);
  WAITV(18); BAR(); stX(6, 0); stW(6, 0);
  WAITV(20); BAR(); stX(7, 1); stW(7, 1);
  WAITV(22); BAR(); stX(8, 2); stW(8, 2);
  int sl = 3;
  for (int t = 4; t < K2_NT - 5; ++t) {
    WAITV(24); BAR();
    stX(t + 5, sl); stW(t + 5, sl);
    if (PF_GU && l == 0) pf_lines<2>((const char*)((j & 1) ? P.wu : P.wg) + ((bid * 2 + (j >> 1)) * 112 + 10 * PF_GU + (t - 4) * 2) * 128);
    sl = sl == K2_NS - 1 ? 0 : sl + 1;
  }
  drain_bar<4, 6>();
  BAR();
}
DI void p2_comp(const Params& P, int l, LAS char* lds, int wid) {
  STAMP_DECL STAMP_AT(0);
  if (wid >= 4) {
#pragma unroll 1
    for (int i = 0; i < K2_NT + 1; ++i) BAR();
    return;
  }
  const int lane = fresh_lane();
  const int bid = fresh_s(blockIdx.x), xcd = bid & 7, loc = bid >> 3;
  const int rt = (xcd & 3) * 4 + (loc & 3), ct = (xcd >> 2) * 8 + (loc >> 2);
  const int m = lane & 15, g = lane >> 4, q = (lane >> 2) & 3, p = lane & 3;
  const int row0 = rt * 128 + wid * 32 + m;
  const unsigned off0 = (unsigned)(row0 * D_ + ct * 64 + 8 * g) * 2;
  int wa[4];
  { const int f = ((q >> 1) & 1) | ((g & 1) << 1);
#pragma unroll
    for (int nb = 0; nb < 4; ++nb) wa[nb] = 16384 + (8 * g + q) * 128 + (((4 * (nb >> 1) + p) ^ (2 * f)) * 16) + (((nb & 1) ^ (p >> 1)) * 8); }
  const int xa = wid * 4096 + lane * 16;
  f32x4 acc[2][4];
  { const brs_t rxi = mk_brs(l ? P.out : P.x0, S_ * D_ * 2);
    u32x4 res[2][2];
#pragma unroll
    for (int mb = 0; mb < 2; ++mb) { res[mb][0] = ldc16(rxi, off0, mb * 32768); res[mb][1] = ldc16(rxi, off0, mb * 32768 + 64); }
#pragma unroll
    for (int mb = 0; mb < 2; ++mb) res_unpack(acc[mb], res[mb], g >= 2, false); }
  bf16x8 xf[2][2][2], wf[2][2][4];
  auto fetch = [&](int set, int sl) {
    LAS char* b = lds + sl * K2_STAGE;
#pragma unroll
    for (int s = 0; s < 2; ++s) {
      xf[set][s][0] = ld128(b + xa + s * 1024); xf[set][s][1] = ld128(b + xa + s * 1024 + 2048);
#pragma unroll
      for (int nb = 0; nb < 4; ++nb) wf[set][s][nb] = ldtr(b + wa[nb] + s * 4096, 512);
    }
  };
  auto mma = [&](int set) {
#pragma unroll
    for (int s = 0; s < 2; ++s)
#pragma unroll
      for (int nb = 0; nb < 4; ++nb)
#pragma unroll
        for (int mb = 0; mb < 2; ++mb) acc[mb][nb] = mfma(wf[set][s][nb], xf[set][s][mb], acc[mb][nb]);
  };
#define K2_MIX() do { _Pragma("unroll") for (int i_ = 0; i_ < 10; ++i_) { SGB(0x8, 1); SGB(0x100, 2); } SGB(0x8, 6); SB0(); } while (0)
  BAR();
  STAMP_AT(1);
  fetch(0, 0);
  SB0();
  int sl = 1;
  for (int t = 1; t < K2_NT - 1; t += 2) {
    WAITL0(); BAR();
    fetch(1, sl); mma(0); K2_MIX();
    sl = sl == K2_NS - 1 ? 0 : sl + 1;
    WAITL0(); BAR();
    fetch(0, sl); mma(1); K2_MIX();
    sl = sl == K2_NS - 1 ? 0 : sl + 1;
  }
  WAITL0(); BAR();
  fetch(1, sl); mma(0); K2_MIX();
  mma(1);
  STAMP_AT(2);
  const brs_t rws = mk_brs(P.ws, WS_BYTES);
#pragma unroll
  for (int mb = 0; mb < 2; ++mb)
    x_store(acc[mb], rws, WS_X1 + off0 + mb * 32768, rws, WS_ST + (unsigned)(2 * l + 1) * 262144u + (ct * 2048 + row0 + mb * 16) * 8, m, g);
  BAR();
  c_signal<true>(P.ws, 64 * l + C_X1 + (rt >> 1), lds, 5 * l + 2, 4);
  STAMP_END(3 + 5 * l);
}

DI void p3_load(const Params& P, int l, LAS char* lds, int j) {
  const int lane = fresh_lane(), tid = j * 64 + lane;
  const int bid = fresh_s(blockIdx.x), xcd = bid & 7, loc = bid >> 3;
  const int rt = (xcd & 1) * 4 + (loc & 3), ct = (xcd >> 1) * 8 + (loc >> 2);
  const int rot = ROT_K3 ? ((loc >> 2) + 2 * (loc & 3)) & 7 : 0;
  const int mt = j & 1, hi2 = j >> 1;
  unsigned voffA, voffB;
  { const int r = lane >> 3, sl8 = lane & 7; voffA = r * 2048 + ((sl8 ^ (r & 6)) * 16);
    const int qq = lane >> 4, sl = lane & 15, f = qq | (hi2 << 2), ch = sl ^ (2 * f) ^ mt; voffB = ch < 14 ? (unsigned)(qq * 7168 + ch * 16) : OOB_V; }
  const rsrc_t rS = mk_rsrc(P.ws, WS_BYTES), rW = mk_rsrc(mt ? P.wu : P.wg, D_ * F_ * 2);
  auto stA = [&](int half, int t) {
    const unsigned sb = WS_X1 + (unsigned)((rt * 256 + half * 128 + j * 32) * 2048 + ((t & 8) | ((t + rot) & 7)) * 128);
#pragma unroll
    for (int u = 0; u < 4; ++u) dma16c(rS, lds + half * 32768 + (t & 1) * 16384 + (4 * j + u) * 1024, voffA, sb + u * 16384);
  };
  auto stB = [&](int s, int t) {
    const unsigned sb = (unsigned)((((t & 8) | ((t + rot) & 7)) * 64 + s * 32 + hi2 * 8) * 7168 + ct * 224);
#pragma unroll
    for (int u = 0; u < 4; ++u) { const int i = (u & 1) + 4 * (u >> 1);
      dma16(rW, lds + 65536 + ((t & 1) * 2 + s) * 16384 + mt * 8192 + (i + 2 * hi2) * 1024, voffB, sb + i * 4 * 7168); }
  };
  stB(0, 0); stB(1, 0); stB(0, 1);
  l_wait(P.ws, 64 * l + C_X1 + rt, 32, lds, 5 * l + 3, j);
  stA(0, 0); stA(1, 0); stA(0, 1);
  u32x2 sv[16]; f32x4 csv;
  { const int t = tid;
    const unsigned so = WS_ST + (unsigned)(2 * l + 1) * 262144u + rt * 2048, vo = t * 8;
#pragma unroll
    for (int i = 0; i < 16; ++i) ALOAD8C(sv[i], rS, vo, so + i * 16384);
    const int ci = t < 224 ? t : 0;
    ALOAD16(csv, rS, (ci + (ci >= 112 ? F_ - 112 : 0)) * 16, WS_CS + (3072 + ct * 112) * 16); }
  WAITV(25); BAR();
  stA(1, 1); WAITV(25); BAR();
  stB(1, 1); WAITV(33); BAR();
  stB(0, 2); WAITV(37); BAR();
  stA(0, 2); WAITV(16); BAR();
  stA(1, 2);
  { const int t = tid;
#pragma unroll
    for (int i = 0; i < 16; ++i) PIN(sv[i]);
    PIN(csv);
    *(LAS f32x2*)(lds + L_STATS + t * 8) = combine_stats<16, false>(sv);
    if (t < 224) *(LAS float*)(lds + L_CS + t * 4) = (csv[0] + csv[1]) + (csv[2] + csv[3]);
    WAITL0(); }
  WAITV(16); BAR();
  stB(1, 2); WAITV(16); BAR();
  stB(0, 3); WAITV(16); BAR();
  stA(0, 3); WAITV(16); BAR();
  for (int t = 2; t < 14; ++t) {
    const char* pfp = (const char*)P.wd + ((bid * 4 + j) * 56 + (t - 2) * 7) * 128;
    const bool pf = l == 0 && t < 10;
    stA(1, t + 1); if (pf) pf_lines<2>(pfp); WAITV(16); BAR();
    stB(1, t + 1); if (pf) pf_lines<2>(pfp + 256); WAITV(16); BAR();
    stB(0, t + 2); if (pf) pf_lines<2>(pfp + 512); WAITV(16); BAR();
    stA(0, t + 2); if (pf) pf_lines<1>(pfp + 768); WAITV(16); BAR();
  }
  stA(1, 15); WAITV(16); BAR();
  stB(1, 15); WAITV(16); BAR();
  drain_bar<3, 4>();
  BAR(); BAR();
  BAR();
}
DI void p3_comp(const Params& P, int l, LAS char* lds, int wid) {
  STAMP_DECL STAMP_AT(0);
  const int bid = fresh_s(blockIdx.x), xcd = bid & 7, loc = bid >> 3;
  const int rt = (xcd & 1) * 4 + (loc & 3), ct = (xcd >> 1) * 8 + (loc >> 2);
  const int grp = wid >> 2, wrl = (wid >> 1) & 1, wc = wid & 1;
  f32x4 acc[4][7];
  {
    const int lane = fresh_lane();
    const int m = lane & 15, g = lane >> 4, q = (lane >> 2) & 3, p = lane & 3;
    int xa[2], wa[7];
#pragma unroll
    for (int s = 0; s < 2; ++s) xa[s] = grp * 32768 + (wrl * 64 + m) * 128 + (((4 * s + g) ^ (m & 6)) * 16);
    { const int f2 = 2 * (q | ((g & 1) << 2)), ro = 65536 + (8 * g + q) * 256, c0 = 7 * wc;
#pragma unroll
      for (int mm = 0; mm < 2; ++mm) {
#pragma unroll
        for (int b = 0; b < 2; ++b) wa[3 * mm + b] = ro + mm * 8192 + (((c0 + p) ^ f2 ^ mm) * 16) + ((b ^ (p >> 1)) * 8);
        wa[3 * mm + 2] = ro + mm * 8192 + (((c0 + 4 + (p >> 1)) ^ f2 ^ mm) * 16) + (p & 1) * 8;
      }
      wa[6] = ro + (p >> 1) * 8192 + (((c0 + 6) ^ f2 ^ (p >> 1)) * 16) + (p & 1) * 8; }
#pragma unroll
    for (int a = 0; a < 4; ++a)
#pragma unroll
      for (int b = 0; b < 7; ++b) acc[a][b] = (f32x4){0.f, 0.f, 0.f, 0.f};
    BAR();
    STAMP_AT(1);
    if (grp == 1) BAR();
    for (int t2 = 0; t2 < 16; t2 += 2) {
#pragma unroll
      for (int buf = 0; buf < 2; ++buf) {
#pragma unroll
        for (int s = 0; s < 2; ++s) {
          bf16x8 xf[4], wf[7];
#pragma unroll
          for (int mb = 0; mb < 4; ++mb) xf[mb] = ld128(lds + xa[s] + buf * 16384 + mb * 2048);
#pragma unroll
          for (int T = 0; T < 7; ++T) wf[T] = ldtr(lds + wa[T] + (buf * 2 + s) * 16384, 1024);
          WAITL0();
          BAR();
          __builtin_amdgcn_s_setprio(1);
#pragma unroll
          for (int mb = 0; mb < 4; ++mb)
#pragma unroll
            for (int T = 0; T < 7; ++T) acc[mb][T] = mfma(wf[T], xf[mb], acc[mb][T]);
          __builtin_amdgcn_s_setprio(0);
          if (buf == 0 || s == 0 || t2 < 14 || grp == 0) BAR();
        }
      }
    }
  }
  STAMP_AT(2);
  const int tz = fresh_lane();
  const int m = tz & 15, g = tz >> 4;
  const LAS f32x2* stt = (const LAS f32x2*)(lds + L_STATS);
  const LAS float* csl = (const LAS float*)(lds + L_CS);
  f32x4 c4[7];
#pragma unroll
  for (int mm = 0; mm < 2; ++mm) {
#pragma unroll
    for (int b = 0; b < 2; ++b) c4[3 * mm + b] = *(const LAS f32x4*)(csl + mm * 112 + wc * 56 + 8 * g + 4 * (b ^ (g >> 1)));
    c4[3 * mm + 2] = *(const LAS f32x4*)(csl + mm * 112 + wc * 56 + 32 + 4 * g);
  }
  c4[6] = *(const LAS f32x4*)(csl + (g >> 1) * 112 + wc * 56 + 48 + 4 * (g & 1));
  const bool hi = g >= 2;
  const brs_t rws = mk_brs(P.ws, WS_BYTES);
  const unsigned hb = WS_H + (unsigned)(rt * 16 + grp * 8 + wrl * 4) * 114688u + (unsigned)(7 * (2 * ct + wc)) * 256u;
  const unsigned v16 = m * 16 + g * 256, v8 = m * 16 + (g >> 1) * 256 + (g & 1) * 8, vz = m * 16 + (g >> 1) * 114688 + (g & 1) * 8;
  f32x4 zv[4];
#pragma unroll
  for (int mb = 0; mb < 4; ++mb) {
    const f32x2 mr = stt[grp * 128 + wrl * 64 + mb * 16 + m];
    u32x2 hv[3];
#pragma unroll
    for (int b = 0; b < 3; ++b) {
      const f32x4 gg = (acc[mb][b] - mr[0] * c4[b]) * mr[1], uu = (acc[mb][3 + b] - mr[0] * c4[3 + b]) * mr[1], hh = gg * uu;
      hv[b][0] = pk2(hh[0], hh[1]); hv[b][1] = pk2(hh[2], hh[3]);
    }
    zv[mb] = (acc[mb][6] - mr[0] * c4[6]) * mr[1];
    const u32x4 o = {hi ? hv[1][0] : hv[0][0], hi ? hv[1][1] : hv[0][1], hi ? hv[0][0] : hv[1][0], hi ? hv[0][1] : hv[1][1]};
    stc16(rws, v16, hb + mb * 114688, o);
    stc8(rws, v8, hb + mb * 114688 + 1024, hv[2]);
  }
#pragma unroll
  for (int mp = 0; mp < 2; ++mp) {
    float hh[4];
#pragma unroll
    for (int e = 0; e < 4; ++e) {
      const auto r = __builtin_amdgcn_permlane32_swap(__float_as_uint(zv[2 * mp][e]), __float_as_uint(zv[2 * mp + 1][e]), false, false);
      hh[e] = __uint_as_float(r[0]) * __uint_as_float(r[1]);
    }
    const u32x2 o = {pk2(hh[0], hh[1]), pk2(hh[2], hh[3])};
    stc8(rws, vz, hb + 2 * mp * 114688 + 1536, o);
  }
  BAR();
  c_signal<true>(P.ws, 64 * l + C_H + rt * 2 + (ct >> 4), lds, 5 * l + 3, 8);
  STAMP_END(4 + 5 * l);
}

constexpr int K4_NT = 28, K4_STAGE = 32768, K4_NS = 4;
DI void p4_load(const Params& P, int l, LAS char* lds, int j) {
  const int lane = fresh_lane();
  const int bid = fresh_s(blockIdx.x), xcd = bid & 7, loc = bid >> 3;
  const int kh = xcd & 1, rt = (xcd >> 1) * 4 + (loc & 3), ct = loc >> 2;
  const int rot = ROT_K4 ? (ct + 2 * (loc & 3)) % 7 : 0;
  const rsrc_t rA = mk_rsrc(P.ws, WS_BYTES), rW = mk_rsrc(P.wd, F_ * D_ * 2);
  const int r = lane >> 3, sl8 = lane & 7, f = ((r >> 1) & 1) | ((j & 1) << 1);
  const unsigned voffW = r * 2048 + ((sl8 ^ (2 * f)) * 16), voffL = lane * 16;
  auto stX = [&](int kt, int sl) {
    const unsigned sa = WS_H + (unsigned)((rt * 8 + 2 * j) * 114688 + kt * 2048);
#pragma unroll
    for (int u = 0; u < 4; ++u) dma16c(rA, lds + sl * K4_STAGE + (4 * j + u) * 1024, voffL, sa + (u >> 1) * 114688 + (u & 1) * 1024);
  };
  auto stW = [&](int kt, int sl) {
    const unsigned sw = (unsigned)((kt * 64 + j * 8) * 2048 + ct * 256);
#pragma unroll
    for (int u = 0; u < 4; ++u) dma16(rW, lds + sl * K4_STAGE + 16384 + (u >> 1) * 8192 + (j + 4 * (u & 1)) * 1024, voffW, sw + (u >> 1) * 128 + (u & 1) * 32 * 2048);
  };
  const int k0 = kh * K4_NT + rot, k1 = kh * K4_NT + (rot + 1) % 7, k2 = kh * K4_NT + (rot + 2) % 7;
  stW(k0, 0); stW(k1, 1); stW(k2, 2);
  l_wait(P.ws, 64 * l + C_H + (rt >> 1) * 2 + kh, 16, lds, 5 * l + 4, j);
  stX(k0, 0); stX(k1, 1); stX(k2, 2);
  int w0 = kh * K4_NT, wr_ = (rot + 3) % 7, sl = 3;
  auto next = [&]() { const int kt = w0 + wr_; stX(kt, sl); stW(kt, sl); wr_ = wr_ == 6 ? 0 : wr_ + 1; if (wr_ == rot) w0 += 7; sl = (sl + 1) & 3; };
  WAITV(8); BAR(); next();
  WAITV(12); BAR(); next();
  for (int t = 2; t < K4_NT - 3; ++t) { WAITV(16); BAR(); next(); }
  drain_bar<2, 8>();
  BAR();
}
DI void p4_comp(const Params& P, int l, LAS char* lds, int wid) {
  STAMP_DECL STAMP_AT(0);
  if (wid >= 4) {
#pragma unroll 1
    for (int i = 0; i < K4_NT + 1; ++i) BAR();
    return;
  }
  const int lane = fresh_lane();
  const int bid = fresh_s(blockIdx.x), xcd = bid & 7, loc = bid >> 3;
  const int kh = xcd & 1, rt = (xcd >> 1) * 4 + (loc & 3), ct = loc >> 2;
  const int wr = wid >> 1, wc = wid & 1;
  const int m = lane & 15, g = lane >> 4, q = (lane >> 2) & 3, p = lane & 3;
  const int row0 = rt * 128 + wr * 64 + kh * 32 + m;
  const unsigned off0 = (unsigned)(row0 * D_ + ct * 128 + wc * 64 + 8 * g) * 2;
  int wa[4];
  { const int f = ((q >> 1) & 1) | ((g & 1) << 1);
#pragma unroll
    for (int nb = 0; nb < 4; ++nb) wa[nb] = 16384 + wc * 8192 + (8 * g + q) * 128 + (((4 * (nb >> 1) + p) ^ (2 * f)) * 16) + (((nb & 1) ^ (p >> 1)) * 8); }
  const int xa0 = wr * 8192 + lane * 16 + kh * 4096, xa1 = wr * 8192 + lane * 16 + 4096 - kh * 4096;
  f32x4 acc[4][4];
#pragma unroll
  for (int a = 0; a < 4; ++a)
#pragma unroll
    for (int b = 0; b < 4; ++b) acc[a][b] = (f32x4){0.f, 0.f, 0.f, 0.f};
  bf16x8 xf[2][4], wf[2][4];
  auto fetch = [&](int s, int sl) {
    LAS char* b = lds + sl * K4_STAGE;
    xf[s][0] = ld128(b + xa0 + s * 1024); xf[s][1] = ld128(b + xa0 + s * 1024 + 2048);
    xf[s][2] = ld128(b + xa1 + s * 1024); xf[s][3] = ld128(b + xa1 + s * 1024 + 2048);
#pragma unroll
    for (int nb = 0; nb < 4; ++nb) wf[s][nb] = ldtr(b + wa[nb] + s * 4096, 512);
  };
  auto mma = [&](int s) {
#pragma unroll
    for (int nb = 0; nb < 4; ++nb)
#pragma unroll
      for (int mb = 0; mb < 4; ++mb) acc[mb][nb] = mfma(wf[s][nb], xf[s][mb], acc[mb][nb]);
  };
#define K4_MIX() do { _Pragma("unroll") for (int i_ = 0; i_ < 12; ++i_) { SGB(0x8, 1); SGB(0x100, 1); } SGB(0x8, 4); SB0(); } while (0)
  const brs_t rws = mk_brs(P.ws, WS_BYTES);
  BAR();
  STAMP_AT(1);
  fetch(0, 0);
  SB0();
  int sl = 0;
  for (int t = 0; t < K4_NT - 1; ++t) {
    fetch(1, sl); mma(0); K4_MIX();
    sl = (sl + 1) & (K4_NS - 1);
    WAITL0(); BAR();
    fetch(0, sl); mma(1); K4_MIX();
  }
  fetch(1, sl); mma(0); K4_MIX();
  mma(1);
  STAMP_AT(2);
  const int pair = rt * 8 + ct;
  u32x4 res[2][2];
#pragma unroll
  for (int mb = 0; mb < 2; ++mb) { res[mb][0] = ldc16(rws, off0, WS_X1 + mb * 32768); res[mb][1] = ldc16(rws, off0, WS_X1 + mb * 32768 + 64); }
  FENCE();
#pragma unroll
  for (int i = 0; i < 4; ++i) { const f32x4 a = acc[2 + (i >> 1)][2 * (i & 1)], b = acc[2 + (i >> 1)][2 * (i & 1) + 1];
    const u32x4 o = {pk2(a[0], a[1]), pk2(a[2], a[3]), pk2(b[0], b[1]), pk2(b[2], b[3])};
    stc16(rws, lane * 16, WS_XCH + pair * 65536 + ((kh ^ 1) * 4 + wid) * 8192 + i * 1024, o); }
  BAR();
#pragma unroll
  for (int mb = 0; mb < 2; ++mb) { res_unpack(acc[mb], res[mb], g >= 2, true); PIN(acc[mb][0]); PIN(acc[mb][1]); PIN(acc[mb][2]); PIN(acc[mb][3]); }
  WAITV(0);
  volatile LAS unsigned* relay = (volatile LAS unsigned*)(lds + L_SYNC + 48);
  { unsigned old = 0;
    if (lane == 0) old = __hip_atomic_fetch_add((LAS unsigned*)(lds + L_SYNC + 52), 1u, __ATOMIC_RELAXED, __HIP_MEMORY_SCOPE_WORKGROUP);
    old = __builtin_amdgcn_readfirstlane(old);
    if ((old & 3) == 3) {
      if (lane == 0) __hip_atomic_store(cnt_ptr(P.ws, C_FLAG + 256 * l + pair * 2 + kh), 1u, __ATOMIC_RELAXED, __HIP_MEMORY_SCOPE_AGENT);
      poll_ge(cnt_ptr(P.ws, C_FLAG + 256 * l + pair * 2 + (kh ^ 1)), 1);
      *relay = l + 1;
    } else { unsigned spins = 0; while ((unsigned)__builtin_amdgcn_readfirstlane(*relay) < (unsigned)(l + 1) && ++spins < (1u << 21)) __builtin_amdgcn_s_sleep(1); }
    FENCE(); }
  u32x4 in[8];
#pragma unroll
  for (int i = 0; i < 4; ++i) in[i] = ldc16(rws, lane * 16, WS_XCH + pair * 65536 + (kh * 4 + wid) * 8192 + i * 1024);
#pragma unroll
  for (int i = 0; i < 4; ++i) { const f32x4 a = {bflo(in[i][0]), bfhi(in[i][0]), bflo(in[i][1]), bfhi(in[i][1])}, b = {bflo(in[i][2]), bfhi(in[i][2]), bflo(in[i][3]), bfhi(in[i][3])};
    acc[i >> 1][2 * (i & 1)] += a; acc[i >> 1][2 * (i & 1) + 1] += b; }
  const brs_t rxo = mk_brs(P.out, S_ * D_ * 2);
#pragma unroll
  for (int mb = 0; mb < 2; ++mb)
    x_store(acc[mb], rxo, off0 + mb * 32768, rws, WS_ST + (unsigned)((2 * l + 2) & 3) * 262144u + ((ct * 2 + wc) * 2048 + row0 + mb * 16) * 8, m, g);
  if (l == 0) c_signal<true>(P.ws, 64 + C_X + rt, lds, 4, 4);
  STAMP_END(5 + 5 * l);
}

DI void init_sync(LAS char* lds) { if (threadIdx.x < 32) *(LAS unsigned*)(lds + L_SYNC + threadIdx.x * 4) = 0u; WAITL0(); }

__global__ __launch_bounds__(768, 3) void k_mega(Params P) {
  extern __shared__ __attribute__((aligned(16))) char smem[];
  LAS char* lds = (LAS char*)smem;
  const int wid = __builtin_amdgcn_readfirstlane(threadIdx.x >> 6);
  if (wid >= 8) {
    const int j = wid & 3;
#pragma unroll 1
    for (int l = 0; l < 2; ++l) { p1_load(P, fresh_s(l), lds, fresh_s(j)); p2_load<true>(P, fresh_s(l), lds, fresh_s(j)); p3_load(P, fresh_s(l), lds, fresh_s(j)); p4_load(P, fresh_s(l), lds, fresh_s(j)); }
  } else {
    init_sync(lds);
#pragma unroll 1
    for (int l = 0; l < 2; ++l) {
      p1_comp(P, fresh_s(l), lds, fresh_s(wid)); p2_comp(P, fresh_s(l), lds, fresh_s(wid));
      p3_comp(P, fresh_s(l), lds, fresh_s(wid)); p4_comp(P, fresh_s(l), lds, fresh_s(wid));
    }
  }
}

extern "C" void kernel_launch(void* const* d_in, const int* in_sizes, int n_in, void* d_out, int out_size, void* d_ws, size_t ws_size, hipStream_t stream) {
  Params P;
  P.x0 = (const u16*)d_in[0]; P.wqkv = (const u16*)d_in[1]; P.wo = (const u16*)d_in[2];
  P.wg = (const u16*)d_in[4]; P.wu = (const u16*)d_in[5]; P.wd = (const u16*)d_in[6];
  P.out = (u16*)d_out; P.ws = (char*)d_ws;
  k0_prep<<<K0_GRID, 256, 0, stream>>>(P);
  (void)hipFuncSetAttribute((const void*)k_mega, hipFuncAttributeMaxDynamicSharedMemorySize, L_BYTES);
  k_mega<<<256, 768, L_BYTES, stream>>>(P);
}
```

```cpp
#include <hip/hip_runtime.h>
#include <hip/hip_bf16.h>
#include <stdint.h>

#define LAS __attribute__((address_space(3)))
#define DI __device__ __forceinline__
typedef unsigned short u16;
typedef __bf16 bf16x8 __attribute__((ext_vector_type(8)));
typedef __bf16 bf16x2 __attribute__((ext_vector_type(2)));
typedef short s16x4 __attribute__((ext_vector_type(4)));
typedef float f32x2 __attribute__((ext_vector_type(2)));
typedef float f32x4 __attribute__((ext_vector_type(4)));
typedef unsigned u32x2 __attribute__((ext_vector_type(2)));
typedef unsigned u32x4 __attribute__((ext_vector_type(4)));
typedef u32x4 rsrc_t;
typedef __amdgpu_buffer_rsrc_t brs_t;

constexpr int S_ = 2048, D_ = 1024, F_ = 3584;
#define MEGA 1
#define QDEF 7
#define SNAKE 1
#define P4_LATEBAR 1
#define X_FULL 1
#define K0_GRID 768
#define K0_NT 1
#define PF_GU 0
#define PF_ON 1
#define POLL_SLEEP 1
#define ROT_K1 0
#define ROT_K3 0
#define ROT_K2 0
#define ROT_K4 1
constexpr unsigned OOB_V = 0x80000000u;
constexpr unsigned WS_PQ = 0, WS_KVP = 4u << 20, WS_X1 = 8u << 20, WS_H = 12u << 20, WS_XCH = 27u << 20, WS_ST = 35u << 20, WS_CS = 36u << 20, WS_CNT = (36u << 20) + (1u << 19),
                   WS_BYTES = 38u << 20;
#define NREP 8
constexpr int C_X = 0, C_KV = 16, C_O = 32, C_X1 = 40, C_H = 48, C_FLAG = 128 * NREP, C_N = C_FLAG + 512;
constexpr int L_STATS = 147456, L_CS = L_STATS + 2048, L_KV = L_CS + 1024, L_SYNC = L_KV + 4224, L_BYTES = L_SYNC + 128;

struct Params { const u16* x0; const u16* wqkv; const u16* wo; const u16* wg; const u16* wu; const u16* wd; u16* out; char* ws; };

DI rsrc_t mk_rsrc(const void* p, unsigned bytes) {
  const unsigned long long a = (unsigned long long)p;
  rsrc_t r = {(unsigned)a, (unsigned)(a >> 32) & 0xffffu, bytes, 0x00020000u};
  return r;
}
DI brs_t mk_brs(const void* p, unsigned bytes) { return __builtin_amdgcn_make_buffer_rsrc((void*)p, 0, bytes, 0x00020000); }
DI void dma16(rsrc_t r, LAS char* dst, unsigned voff, unsigned soff) {
  asm volatile("s_mov_b32 m0, %0\n\ts_nop 0\n\tbuffer_load_dwordx4 %1, %2, %3 offen lds" :: "s"((unsigned)(size_t)dst), "v"(voff), "s"(r), "s"(soff) : "memory");
}
DI void dma16c(rsrc_t r, LAS char* dst, unsigned voff, unsigned soff) {
  asm volatile("s_mov_b32 m0, %0\n\ts_nop 0\n\tbuffer_load_dwordx4 %1, %2, %3 offen sc1 lds" :: "s"((unsigned)(size_t)dst), "v"(voff), "s"(r), "s"(soff) : "memory");
}
DI u32x4 ldc16(brs_t r, unsigned voff, unsigned soff) { return __builtin_amdgcn_raw_buffer_load_b128(r, voff, soff, 16); }
DI void stc16(brs_t r, unsigned voff, unsigned soff, u32x4 v) { __builtin_amdgcn_raw_buffer_store_b128(v, r, voff, soff, 16); }
DI void stc16(brs_t r, unsigned voff, unsigned soff, f32x4 v) { stc16(r, voff, soff, __builtin_bit_cast(u32x4, v)); }
DI void stc8(brs_t r, unsigned voff, unsigned soff, u32x2 v) { __builtin_amdgcn_raw_buffer_store_b64(v, r, voff, soff, 16); }
DI f32x4 mfma(bf16x8 a, bf16x8 b, f32x4 c) { return __builtin_amdgcn_mfma_f32_16x16x32_bf16(a, b, c, 0, 0, 0); }
DI bf16x8 ld128(LAS char* p) { return *(LAS bf16x8*)p; }
DI bf16x8 ldtr(LAS char* p, int off2) {
  s16x4 a = __builtin_amdgcn_ds_read_tr16_b64_v4i16((LAS s16x4*)p);
  s16x4 b = __builtin_amdgcn_ds_read_tr16_b64_v4i16((LAS s16x4*)(p + off2));
  return __builtin_bit_cast(bf16x8, __builtin_shufflevector(a, b, 0, 1, 2, 3, 4, 5, 6, 7));
}
DI unsigned pk2(float a, float b) { f32x2 v = {a, b}; return __builtin_bit_cast(unsigned, __builtin_convertvector(v, bf16x2)); }
DI float bflo(unsigned u) { return __uint_as_float(u << 16); }
DI float bfhi(unsigned u) { return __uint_as_float(u & 0xffff0000u); }
DI float phi_ln(float u, float rs, float rs2) {
  float e; asm("v_exp_f32 %0, %1 clamp" : "=v"(e) : "v"(u * rs2));
  return fmaxf(__builtin_fmaf(u, rs, 1e-6f), 1e-6f) + e;
}
DI float xsum(float s) {
  const auto a = __builtin_amdgcn_permlane16_swap(__float_as_uint(s), __float_as_uint(s), false, false);
  s = __uint_as_float(a[0]) + __uint_as_float(a[1]);
  const auto b = __builtin_amdgcn_permlane32_swap(__float_as_uint(s), __float_as_uint(s), false, false);
  return __uint_as_float(b[0]) + __uint_as_float(b[1]);
}
#define SGB(mask, n) __builtin_amdgcn_sched_group_barrier(mask, n, 0)
#define SB0() __builtin_amdgcn_sched_barrier(0)
#define PIN(x) asm volatile("" : "+v"(x))
#define FENCE() asm volatile("" ::: "memory")
#define BAR() do { FENCE(); __builtin_amdgcn_s_barrier(); FENCE(); __builtin_amdgcn_sched_barrier(0); } while (0)
#define WAITV(n) asm volatile("s_waitcnt vmcnt(%0)" :: "i"(n) : "memory")
#define WAITL0() do { FENCE(); __builtin_amdgcn_s_waitcnt(0xc07f); FENCE(); } while (0)
DI int fresh_s(int x) { asm volatile("" : "+s"(x)); return x; }
DI int fresh_lane() { int t; asm volatile("v_mbcnt_lo_u32_b32 %0, -1, 0\n\tv_mbcnt_hi_u32_b32 %0, -1, %0" : "=v"(t)); return t; }

#define STAMP_DECL
#define STAMP_AT(i)
#define STAMP_END(sl_)

template <int N> DI void pf_lines(const void* p) {
#pragma unroll
  for (int i = 0; i < 2 * N; ++i) asm volatile("s_load_dword s100, %0, %1" :: "s"(p), "i"(i * 64) : "s100");
}
DI unsigned* cnt_ptr(char* ws, int idx) { return (unsigned*)(ws + WS_CNT + idx * 128); }
DI unsigned* rep_ptr(char* ws, int idx) { return cnt_ptr(ws, idx * NREP + ((blockIdx.x >> 3) + (blockIdx.x & 7) * 5) % NREP); }
DI void poll_ge(unsigned* c, unsigned target) {
  unsigned spins = 0;
  while ((unsigned)__builtin_amdgcn_readfirstlane(__hip_atomic_load(c, __ATOMIC_RELAXED, __HIP_MEMORY_SCOPE_AGENT)) < target && ++spins < (1u << 21)) __builtin_amdgcn_s_sleep(POLL_SLEEP);
  FENCE();
}
DI void l_wait(char* ws, int idx, unsigned target, LAS char* lds, unsigned epoch, int j) {
  if (!MEGA) return;
  volatile LAS unsigned* relay = (volatile LAS unsigned*)(lds + L_SYNC);
  if (j == 0) { poll_ge(rep_ptr(ws, idx), target); *relay = epoch; }
  else { unsigned spins = 0; while ((unsigned)__builtin_amdgcn_readfirstlane(*relay) < epoch && ++spins < (1u << 24)) __builtin_amdgcn_s_sleep(1); }
  FENCE();
}
template <bool REP> DI void c_signal(char* ws, int idx, LAS char* lds, int arr, unsigned nw) {
  if (!MEGA) return;
  WAITV(0);
  const int lane = fresh_lane();
  unsigned old = 0;
  if (lane == 0) old = __hip_atomic_fetch_add((LAS unsigned*)(lds + L_SYNC + 4 + arr * 4), 1u, __ATOMIC_RELAXED, __HIP_MEMORY_SCOPE_WORKGROUP);
  if ((unsigned)__builtin_amdgcn_readfirstlane(old) == nw - 1 && lane < (REP ? NREP : 1))
    __hip_atomic_fetch_add(cnt_ptr(ws, REP ? idx * NREP + lane : idx), 1u, __ATOMIC_RELAXED, __HIP_MEMORY_SCOPE_AGENT);
}

template <int NT, bool PAIR> DI f32x2 combine_stats(const u32x2 (&u)[NT]) {
  float s = 0.f;
#pragma unroll
  for (int i = 0; i < NT; ++i) s += __uint_as_float(u[i][0]);
  if (PAIR) s += __shfl_xor(s, 1);
  const float mean = s * (1.f / 1024.f);
  float q = 0.f;
#pragma unroll
  for (int i = 0; i < NT; ++i) { const float d = __uint_as_float(u[i][0]) * (1.f / 64.f) - mean; q += __uint_as_float(u[i][1]) + 64.f * d * d; }
  if (PAIR) q += __shfl_xor(q, 1);
  f32x2 r; r[0] = mean; r[1] = __builtin_amdgcn_rsqf(q * (1.f / 1024.f) + 1e-5f);
  return r;
}
DI u32x4 ones_bf16() { unsigned o = 0x3f803f80u; asm volatile("" : "+v"(o)); const u32x4 r = {o, o, o, o}; return r; }
DI void res_unpack(f32x4 (&a)[4], const u32x4 (&res)[2], bool hi, bool add) {
#pragma unroll
  for (int pr = 0; pr < 2; ++pr) {
    const unsigned u0 = hi ? res[pr][2] : res[pr][0], u1 = hi ? res[pr][3] : res[pr][1], u2 = hi ? res[pr][0] : res[pr][2], u3 = hi ? res[pr][1] : res[pr][3];
    const f32x4 x = {bflo(u0), bfhi(u0), bflo(u1), bfhi(u1)}, y = {bflo(u2), bfhi(u2), bflo(u3), bfhi(u3)};
    if (add) { a[2 * pr] += x; a[2 * pr + 1] += y; } else { a[2 * pr] = x; a[2 * pr + 1] = y; }
  }
}
DI void x_store(const f32x4 (&a)[4], brs_t rxo, unsigned xoff, brs_t rws, unsigned stoff, int m, int g, bool stats) {
  const bool hi = g >= 2;
  const u32x4 ones4 = ones_bf16();
  f32x4 sm = {0.f, 0.f, 0.f, 0.f}, sq = {0.f, 0.f, 0.f, 0.f};
  u32x4 o[2];
#pragma unroll
  for (int pr = 0; pr < 2; ++pr) {
    const unsigned p0 = pk2(a[2 * pr][0], a[2 * pr][1]), p1 = pk2(a[2 * pr][2], a[2 * pr][3]), p2 = pk2(a[2 * pr + 1][0], a[2 * pr + 1][1]), p3 = pk2(a[2 * pr + 1][2], a[2 * pr + 1][3]);
    o[pr][0] = hi ? p2 : p0; o[pr][1] = hi ? p3 : p1; o[pr][2] = hi ? p0 : p2; o[pr][3] = hi ? p1 : p3;
  }
  u32x4 sa, sb;
#pragma unroll
  for (int e = 0; e < 4; ++e) {
    sa[e] = __builtin_amdgcn_update_dpp(o[0][e], o[1][e], 0x128, 0xf, 0xc, false);
    sb[e] = __builtin_amdgcn_update_dpp(o[1][e], o[0][e], 0x128, 0xf, 0x3, false);
  }
  stc16(rxo, xoff, 0, sa); stc16(rxo, xoff, 8 * D_ * 2, sb);
  if (!stats) return;
#pragma unroll
  for (int pr = 0; pr < 2; ++pr) {
    const bf16x8 f = __builtin_bit_cast(bf16x8, o[pr]);
    sm = mfma(__builtin_bit_cast(bf16x8, ones4), f, sm);
    sq = mfma(f, f, sq);
  }
  const float dq = (m & 2) ? ((m & 1) ? sq[3] : sq[2]) : ((m & 1) ? sq[1] : sq[0]);
  if (g == (m >> 2)) { const u32x2 r = {__float_as_uint(sm[0]), __float_as_uint(fmaxf(dq - sm[0] * sm[0] * (1.f / 64.f), 0.f))}; stc8(rws, stoff, 0, r); }
}
DI unsigned xs_adj(int m) { return X_FULL ? (unsigned)((m >> 3) * (64 - 8 * D_ * 2)) : 0u; }
template <int N, int NI> DI void drain_bar() { WAITV(N * NI); BAR(); if constexpr (N > 0) drain_bar<N - 1, NI>(); }

__global__ __launch_bounds__(256) void k0_prep(Params P) {
  __shared__ float red[32][65];
  STAMP_DECL STAMP_AT(0); STAMP_AT(1); STAMP_AT(2);
  const int b = blockIdx.x, t = threadIdx.x;
  float* stats = (float*)(P.ws + WS_ST); float* cs = (float*)(P.ws + WS_CS);
  if (b * 256 + t < C_N) *cnt_ptr(P.ws, b * 256 + t) = 0u;
  if (b < 128) {
    const int row = b * 16 + (t >> 4), tile = t & 15;
    const u32x4* p = (const u32x4*)(P.x0 + (size_t)row * D_ + tile * 64);
    u32x4 v[8];
#pragma unroll
    for (int i = 0; i < 8; ++i) v[i] = p[i];
    float s = 0.f;
#pragma unroll
    for (int i = 0; i < 8; ++i)
#pragma unroll
      for (int j = 0; j < 4; ++j) s += bflo(v[i][j]) + bfhi(v[i][j]);
    const float m = s * (1.f / 64.f); float q = 0.f;
#pragma unroll
    for (int i = 0; i < 8; ++i)
#pragma unroll
      for (int j = 0; j < 4; ++j) { const float a = bflo(v[i][j]) - m, c = bfhi(v[i][j]) - m; q += a * a + c * c; }
    f32x2 r = {s, q};
    *(f32x2*)(stats + ((size_t)tile * 2048 + row) * 2) = r;
  } else if (b < K0_GRID) {
    const int pn = (b - 128) >> 2, kq = (b - 128) & 3;
    const u16* w; int ld, col, outoff;
    if (pn < 48) { w = P.wqkv; ld = 3072; col = pn * 64; outoff = col; }
    else if (pn < 104) { w = P.wg; ld = F_; col = (pn - 48) * 64; outoff = 3072 + col; }
    else { w = P.wu; ld = F_; col = (pn - 104) * 64; outoff = 3072 + F_ + col; }
    const int c8 = t & 7, r0 = t >> 3;
    const u32x4* src = (const u32x4*)(w + (size_t)(kq * 256 + r0) * ld + col + c8 * 8);
    u32x4 v[8];
    if (K0_NT == 2 || (K0_NT && pn >= 48)) {
#pragma unroll
      for (int i = 0; i < 8; ++i) v[i] = __builtin_nontemporal_load(src + (size_t)i * 4 * ld);
    } else {
#pragma unroll
      for (int i = 0; i < 8; ++i) v[i] = src[(size_t)i * 4 * ld];
    }
    float a[8] = {0, 0, 0, 0, 0, 0, 0, 0};
#pragma unroll
    for (int i = 0; i < 8; ++i)
#pragma unroll
      for (int j = 0; j < 4; ++j) { a[2 * j] += bflo(v[i][j]); a[2 * j + 1] += bfhi(v[i][j]); }
#pragma unroll
    for (int j = 0; j < 8; ++j) red[r0][c8 * 8 + j] = a[j];
    __syncthreads();
    if (t < 64) { float s = 0.f;
#pragma unroll
      for (int r = 0; r < 32; ++r) s += red[r][t];
      cs[(outoff + t) * 4 + kq] = s; }
  }
#define STAMP_WID __builtin_amdgcn_readfirstlane(threadIdx.x >> 6)
  STAMP_END(0);
#undef STAMP_WID
#define STAMP_WID wid
}

#define ALOAD8C(dst, rs, voff, soff) asm volatile("buffer_load_dwordx2 %0, %1, %2, %3 offen sc1" : "=v"(dst) : "v"(voff), "s"(rs), "s"(soff) : "memory")
#define ALOAD16(dst, rs, voff, soff) asm volatile("buffer_load_dwordx4 %0, %1, %2, %3 offen" : "=v"(dst) : "v"(voff), "s"(rs), "s"(soff) : "memory")

constexpr int K1_STAGE = 40960, K1_RED = 3 * K1_STAGE, K1_D = QDEF;
DI void p1_load(const Params& P, int l, LAS char* lds, int j) {
  const int lane = fresh_lane(), tid = j * 64 + lane;
  const int bid = fresh_s(blockIdx.x), xcd = bid & 7, loc = bid >> 3;
  const int rt = (xcd & 1) * 8 + (loc & 7), hp = (xcd >> 1) * 4 + (loc >> 3);
  const int rot = ROT_K1 ? ((loc & 7) + 2 * (loc >> 3)) & 7 : 0;
  const rsrc_t rX = mk_rsrc(l ? P.out : P.x0, S_ * D_ * 2), rW = mk_rsrc(P.wqkv, D_ * 3072 * 2), rS = mk_rsrc(P.ws, WS_BYTES);
  const int r = lane >> 3, sl8 = lane & 7, f = ((r >> 1) & 1) | ((j & 1) << 1);
  const unsigned voffX = r * 2048 + ((sl8 ^ (r & 6)) * 16), voffW = r * 6144 + ((sl8 ^ (2 * f)) * 16);
  auto stX = [&](int t, int st) {
    const unsigned sx = (unsigned)((rt * 128 + j * 32) * 2048 + ((t & 8) | ((t + rot) & 7)) * 128);
#pragma unroll
    for (int u = 0; u < 4; ++u) dma16c(rX, lds + st * K1_STAGE + (4 * j + u) * 1024, voffX, sx + u * 16384);
  };
  auto stW = [&](int t, int st, int p0 = 0, int p1 = 3) {
    const unsigned sw = (unsigned)((((t & 8) | ((t + rot) & 7)) * 64 + j * 8) * 6144 + hp * 128);
#pragma unroll
    for (int s = 0; s < 2; ++s)
#pragma unroll
      for (int pnl = p0; pnl < p1; ++pnl) dma16(rW, lds + st * K1_STAGE + 16384 + pnl * 8192 + (4 * s + j) * 1024, voffW, sw + pnl * 2048 + s * 32 * 6144);
  };
  stW(0, 0); stW(1, 1);
  if (l) l_wait(P.ws, 64 + C_X + rt, 16, lds, 5, j);
  stX(0, 0); stX(1, 1);
  u32x2 sv[8]; f32x4 csv;
  { const int t = tid;
    const unsigned so = WS_ST + (unsigned)(2 * l) * 262144u + rt * 1024, vo = (t & 1) * 131072 + (t >> 1) * 8;
#pragma unroll
    for (int i = 0; i < 8; ++i) ALOAD8C(sv[i], rS, vo, so + i * 16384);
    const int ci = t < 192 ? t : 0;
    ALOAD16(csv, rS, ((ci >> 6) * 1024 + (ci & 63)) * 16, WS_CS + hp * 1024); }
  WAITV(13); BAR();
  stX(2, 2); stW(2, 2);
  WAITV(19); BAR();
  stX(3, 0); stW(3, 0);
  WAITV(10); BAR();
  stX(4, 1); stW(4, 1);
  { const int t = tid;
#pragma unroll
    for (int i = 0; i < 8; ++i) PIN(sv[i]);
    PIN(csv);
    const f32x2 mr = combine_stats<8, true>(sv);
    if (!(t & 1)) *(LAS f32x2*)(lds + L_STATS + (t >> 1) * 8) = mr;
    if (t < 192) *(LAS float*)(lds + L_CS + t * 4) = (csv[0] + csv[1]) + (csv[2] + csv[3]);
    WAITL0(); }
  WAITV(10); BAR();
  int st = 2;
  for (int t = 3; t < 14 - K1_D; ++t) {
    stX(t + 2, st); stW(t + 2, st);
    if (l == 0) pf_lines<4>((const char*)P.wo + ((bid * 4 + j) * 16 + ((t - 3) & 3) * 4) * 128);
    WAITV(10); BAR();
    st = st == 2 ? 0 : st + 1;
  }
  for (int t = 14 - K1_D; t < 14; ++t) {
    stX(t + 2, st); stW(t + 2, st, 1, 3);
    WAITV(8); BAR();
    st = st == 2 ? 0 : st + 1;
  }
  for (int t = 14; t < 14 + K1_D; ++t) {
    stX(t + 2 - K1_D, st); stW(t + 2 - K1_D, st, 0, 1);
    if (t == 16) BAR();
    WAITV(6); BAR();
    st = st == 2 ? 0 : st + 1;
  }
  WAITV(0); BAR();
  BAR(); BAR();
}
DI void p1_comp(const Params& P, int l, LAS char* lds, int wid) {
  STAMP_DECL STAMP_AT(0);
  const int bid = fresh_s(blockIdx.x), xcd = bid & 7, loc = bid >> 3;
  const int rt = (xcd & 1) * 8 + (loc & 7), hp = (xcd >> 1) * 4 + (loc >> 3);
  if (wid >= 4) {
#pragma unroll 1
    for (int i = 0; i < 16 + K1_D; ++i) BAR();
    unsigned look = 0;
    if (wid == 4) asm volatile("global_load_dword %0, %1, %2 sc1" : "=v"(look) : "v"(0), "s"(rep_ptr(P.ws, 64 * l + C_KV + hp)) : "memory");
    BAR();
    STAMP_AT(0);
    volatile LAS unsigned* relay = (volatile LAS unsigned*)(lds + L_SYNC + 56);
    if (wid == 4) {
      asm volatile("s_waitcnt vmcnt(0)" : "+v"(look) :: "memory");
      if ((unsigned)__builtin_amdgcn_readfirstlane(look) < 16u) poll_ge(rep_ptr(P.ws, 64 * l + C_KV + hp), 16);
      *relay = l + 1;
    }
    else { unsigned spins = 0; while ((unsigned)__builtin_amdgcn_readfirstlane(*relay) < (unsigned)(l + 1) && ++spins < (1u << 24)) __builtin_amdgcn_s_sleep(1); }
    FENCE();
    STAMP_AT(1);
    const int lane = fresh_lane(), hw = (wid >> 1) & 1, wr = wid & 1;
    const brs_t rws = mk_brs(P.ws, WS_BYTES);
    const unsigned src = WS_KVP + (2 * hp + hw) * 2176;
    u32x4 v[16];
#pragma unroll
    for (int i = 0; i < 16; ++i) v[i] = ldc16(rws, lane * 16, src + i * 32 * 2176 + wr * 1024);
    const u32x2 zv = __builtin_amdgcn_raw_buffer_load_b64(rws, (lane >> 2) * (32 * 2176) + (lane & 3) * 8, src + 2048 + wr * 32, 16);
    float s[8] = {0.f, 0.f, 0.f, 0.f, 0.f, 0.f, 0.f, 0.f};
#pragma unroll
    for (int i = 0; i < 16; ++i)
#pragma unroll
      for (int e = 0; e < 4; ++e) { s[2 * e] += bflo(v[i][e]); s[2 * e + 1] += bfhi(v[i][e]); }
#pragma unroll
    for (int b = 0; b < 2; ++b) {
      const int e = 16 * b + (lane & 15), i16 = (e >> 3) * 4 + (e & 3), bp = (e >> 2) & 1;
      const u32x2 o = {pk2(s[4 * b], s[4 * b + 1]), pk2(s[4 * b + 2], s[4 * b + 3])};
      *(LAS u32x2*)(lds + L_KV + hw * 2112 + bp * 1024 + ((lane >> 4) * 16 + i16) * 16 + 8 * wr) = o;
    }
    float z[4] = {bflo(zv[0]), bfhi(zv[0]), bflo(zv[1]), bfhi(zv[1])};
#pragma unroll
    for (int e = 0; e < 4; ++e) {
      z[e] += __uint_as_float(__builtin_amdgcn_update_dpp(0u, __float_as_uint(z[e]), 0x124, 0xf, 0xf, false));
      z[e] += __uint_as_float(__builtin_amdgcn_update_dpp(0u, __float_as_uint(z[e]), 0x128, 0xf, 0xf, false));
      z[e] = xsum(z[e]);
    }
    if (lane < 4) { const u32x2 oz = {pk2(z[0], z[1]), pk2(z[2], z[3])}; *(LAS u32x2*)(lds + L_KV + hw * 2112 + 2048 + lane * 16 + 8 * wr) = oz; }
    WAITL0();
    BAR(); BAR();
    return;
  }
  const int wr = wid >> 1, wc = wid & 1;
  f32x4 acc[3][4][2];
  {
    const int lane = fresh_lane(), m = lane & 15, g = lane >> 4, q = (lane >> 2) & 3, p = lane & 3;
    int xa0 = (wr * 64 + m) * 128 + ((g ^ (m & 6)) * 16), xa1 = xa0 ^ 64;
    int wa0 = 16384 + (8 * g + q) * 128 + (((4 * wc + (p >> 1)) ^ (2 * (((q >> 1) & 1) | ((g & 1) << 1)))) * 16) + (p & 1) * 8, wa1 = wa0 ^ 32;
#pragma unroll
    for (int a = 0; a < 3; ++a)
#pragma unroll
      for (int b = 0; b < 4; ++b)
#pragma unroll
        for (int c = 0; c < 2; ++c) acc[a][b][c] = (f32x4){0.f, 0.f, 0.f, 0.f};
    bf16x8 xf[4], wf[2][3][2];
    auto step = [&](int cur, bool q_now, bool q_next) {
      LAS char* xp = lds + (cur ? xa0 : xa1); LAS char* w0 = lds + wa0 + (cur ? 0 : 4096); LAS char* w1 = lds + wa1 + (cur ? 0 : 4096);
#pragma unroll
      for (int mb = 0; mb < 4; ++mb) {
        const bf16x8 x = xf[mb];
        if (q_next) {
          if (mb == 1) { wf[cur ^ 1][0][0] = ldtr(w0, 512); xf[0] = ld128(xp); wf[cur ^ 1][1][0] = ldtr(w0 + 8192, 512); }
          if (mb == 2) { wf[cur ^ 1][2][0] = ldtr(w0 + 16384, 512); xf[1] = ld128(xp + 2048); wf[cur ^ 1][0][1] = ldtr(w1, 512); }
          if (mb == 3) { wf[cur ^ 1][1][1] = ldtr(w1 + 8192, 512); xf[2] = ld128(xp + 4096); wf[cur ^ 1][2][1] = ldtr(w1 + 16384, 512); }
        } else {
          if (mb == 1) { wf[cur ^ 1][1][0] = ldtr(w0 + 8192, 512); wf[cur ^ 1][2][0] = ldtr(w0 + 16384, 512); }
          if (mb == 2) { xf[0] = ld128(xp); wf[cur ^ 1][1][1] = ldtr(w1 + 8192, 512); xf[1] = ld128(xp + 2048); }
          if (mb == 3) { wf[cur ^ 1][2][1] = ldtr(w1 + 16384, 512); xf[2] = ld128(xp + 4096); }
        }
#pragma unroll
        for (int nb = 0; nb < 2; ++nb) {
          if (q_now) acc[0][mb][nb] = mfma(wf[cur][0][nb], x, acc[0][mb][nb]);
          acc[1][mb][nb] = mfma(x, wf[cur][1][nb], acc[1][mb][nb]);
          acc[2][mb][nb] = mfma(x, wf[cur][2][nb], acc[2][mb][nb]);
        }
        if (mb == 0 && cur) { SB0(); WAITL0(); BAR(); }
        if (mb == 3) xf[3] = ld128(xp + 6144);
        if (mb > 0) { _Pragma("unroll") for (int i_ = 0; i_ < 6; ++i_) { SGB(0x8, 1); SGB(0x100, 1); } }
        SB0();
      }
    };
    auto advance = [&](int& st) {
      const int d = st == 2 ? -2 * K1_STAGE : K1_STAGE; st = st == 2 ? 0 : st + 1;
      xa0 += d; xa1 += d; wa0 += d; wa1 += d; PIN(xa0); PIN(xa1); PIN(wa0); PIN(wa1);
    };
    BAR();
    STAMP_AT(1);
#pragma unroll
    for (int mb = 0; mb < 4; ++mb) xf[mb] = ld128(lds + xa0 + mb * 2048);
#pragma unroll
    for (int pnl = 0; pnl < 3; ++pnl) { wf[0][pnl][0] = ldtr(lds + wa0 + pnl * 8192, 512); wf[0][pnl][1] = ldtr(lds + wa1 + pnl * 8192, 512); }
    SB0();
    int st = 0;
    for (int t = 0; t < 16 - K1_D; ++t) { step(0, true, true); advance(st); step(1, true, true); }
    for (int t = 16 - K1_D; t < 16; ++t) { step(0, false, false); advance(st); step(1, false, false); }
  }
  STAMP_AT(2);
  const int lane = fresh_lane(), m = lane & 15, g = lane >> 4;
  const brs_t rws = mk_brs(P.ws, WS_BYTES);
  const int head = 2 * hp + wc, trow = wr * 64;
  const LAS f32x2* stt = (const LAS f32x2*)(lds + L_STATS);
  const LAS float* csl = (const LAS float*)(lds + L_CS);
  const u32x4 ones4 = ones_bf16();
  f32x4 kvr[6];
#pragma unroll
  for (int i = 0; i < 6; ++i) kvr[i] = (f32x4){0.f, 0.f, 0.f, 0.f};
  float ck[2], cv[2];
#pragma unroll
  for (int nb = 0; nb < 2; ++nb) { ck[nb] = csl[64 + wc * 32 + nb * 16 + m]; cv[nb] = csl[128 + wc * 32 + nb * 16 + m]; }
#pragma unroll
  for (int mp = 0; mp < 2; ++mp) {
    u32x4 pkf[2], vf[2];
#pragma unroll
    for (int hb = 0; hb < 2; ++hb) {
      const int mb = 2 * mp + hb;
      const f32x4 a0 = *(const LAS f32x4*)(stt + trow + mb * 16 + 4 * g), a1 = *(const LAS f32x4*)(stt + trow + mb * 16 + 4 * g + 2);
      const float mean[4] = {a0[0], a0[2], a1[0], a1[2]}, rstd[4] = {a0[1], a0[3], a1[1], a1[3]};
#pragma unroll
      for (int nb = 0; nb < 2; ++nb) {
        float kk[4], vv[4];
#pragma unroll
        for (int e = 0; e < 4; ++e) { kk[e] = phi_ln(acc[1][mb][nb][e] - mean[e] * ck[nb], rstd[e], rstd[e] * 1.44269504f); vv[e] = rstd[e] * (acc[2][mb][nb][e] - mean[e] * cv[nb]); }
        pkf[nb][2 * hb] = pk2(kk[0], kk[1]); pkf[nb][2 * hb + 1] = pk2(kk[2], kk[3]);
        vf[nb][2 * hb] = pk2(vv[0], vv[1]); vf[nb][2 * hb + 1] = pk2(vv[2], vv[3]);
      }
      SB0();
    }
#pragma unroll
    for (int a = 0; a < 2; ++a) {
#pragma unroll
      for (int b = 0; b < 2; ++b) kvr[a * 3 + b] = mfma(__builtin_bit_cast(bf16x8, pkf[a]), __builtin_bit_cast(bf16x8, vf[b]), kvr[a * 3 + b]);
      kvr[a * 3 + 2] = mfma(__builtin_bit_cast(bf16x8, pkf[a]), __builtin_bit_cast(bf16x8, ones4), kvr[a * 3 + 2]);
    }
  }
  f32x4 own[3];
#pragma unroll
  for (int i = 0; i < 3; ++i) {
    f32x4 snd;
#pragma unroll
    for (int e = 0; e < 4; ++e) { snd[e] = wr ? kvr[i][e] : kvr[3 + i][e]; own[i][e] = wr ? kvr[3 + i][e] : kvr[i][e]; }
    *(LAS f32x4*)(lds + K1_RED + wid * 3072 + i * 1024 + lane * 16) = snd;
  }
  WAITL0();
  BAR();
#pragma unroll
  for (int i = 0; i < 3; ++i) own[i] += *(LAS f32x4*)(lds + K1_RED + (wid ^ 2) * 3072 + i * 1024 + lane * 16);
  { const unsigned dst = WS_KVP + (rt * 32 + head) * 2176;
    const u32x4 o = {pk2(own[0][0], own[0][1]), pk2(own[0][2], own[0][3]), pk2(own[1][0], own[1][1]), pk2(own[1][2], own[1][3])};
    stc16(rws, lane * 16, dst + wr * 1024, o);
    const u32x2 oz = {pk2(own[2][0], own[2][1]), pk2(own[2][2], own[2][3])};
    if (m == 0) stc8(rws, g * 8, dst + 2048 + wr * 32, oz); }
  { const int q = (lane >> 2) & 3, p = lane & 3;
    const int xa0 = (wr * 64 + m) * 128 + ((g ^ (m & 6)) * 16), xa1 = xa0 ^ 64;
    const int wa0 = 16384 + (8 * g + q) * 128 + (((4 * wc + (p >> 1)) ^ (2 * (((q >> 1) & 1) | ((g & 1) << 1)))) * 16) + (p & 1) * 8, wa1 = wa0 ^ 32;
    bf16x8 xg[2][2][4], wg[2][2][2];
    auto fetchq = [&](int set, int st) {
      LAS char* b = lds + st * K1_STAGE;
#pragma unroll
      for (int s = 0; s < 2; ++s) {
        wg[set][s][0] = ldtr(b + wa0 + s * 4096, 512); wg[set][s][1] = ldtr(b + wa1 + s * 4096, 512);
#pragma unroll
        for (int mb = 0; mb < 4; ++mb) xg[set][s][mb] = ld128(b + (s ? xa1 : xa0) + mb * 2048);
      }
    };
    auto mmaq = [&](int set) {
#pragma unroll
      for (int s = 0; s < 2; ++s)
#pragma unroll
        for (int mb = 0; mb < 4; ++mb)
#pragma unroll
          for (int nb = 0; nb < 2; ++nb) acc[0][mb][nb] = mfma(wg[set][s][nb], xg[set][s][mb], acc[0][mb][nb]);
    };
#define K1Q_MIX() do { _Pragma("unroll") for (int i_ = 0; i_ < 8; ++i_) { SGB(0x8, 1); SGB(0x100, 2); } SGB(0x8, 8); SB0(); } while (0)
    fetchq(0, 16 % 3);
    SB0();
    c_signal<true>(P.ws, 64 * l + C_KV + hp, lds, 5 * l + 0, 4);
#pragma unroll
    for (int u = 1; u < K1_D; ++u) { WAITL0(); BAR(); fetchq(u & 1, (16 + u) % 3); mmaq((u & 1) ^ 1); K1Q_MIX(); }
    mmaq((K1_D - 1) & 1); }
  f32x4 c4[2];
#pragma unroll
  for (int nb = 0; nb < 2; ++nb) c4[nb] = *(const LAS f32x4*)(csl + wc * 32 + nb * 16 + 4 * g);
  u32x4 pq[4];
#pragma unroll
  for (int mb = 0; mb < 4; ++mb) {
    const f32x2 mr = stt[trow + mb * 16 + m];
    const float rs2 = mr[1] * 1.44269504f;
#pragma unroll
    for (int nb = 0; nb < 2; ++nb) {
      f32x4 v;
#pragma unroll
      for (int e = 0; e < 4; ++e) v[e] = phi_ln(acc[0][mb][nb][e] - mr[0] * c4[nb][e], mr[1], rs2);
      pq[mb][2 * nb] = pk2(v[0], v[1]); pq[mb][2 * nb + 1] = pk2(v[2], v[3]);
    }
    PIN(pq[mb]);
  }
  STAMP_END(1 + 5 * l);
  STAMP_AT(0);
  BAR();
  STAMP_AT(1); STAMP_AT(2);
  { LAS char* kb = lds + L_KV + wc * 2112;
    const bf16x8 k0 = ld128(kb + lane * 16), k1 = ld128(kb + 1024 + lane * 16), zf = ld128(kb + 2048 + g * 16);
    const f32x4 z4 = {0.f, 0.f, 0.f, 0.f};
#pragma unroll
    for (int mb = 0; mb < 4; ++mb) {
      const bf16x8 x = __builtin_bit_cast(bf16x8, pq[mb]);
      const f32x4 dn = mfma(zf, x, z4), d0 = mfma(k0, x, z4), d1 = mfma(k1, x, z4);
      const float rr = __builtin_amdgcn_rcpf(dn[0] + 1e-6f);
      const u32x4 o = {pk2(d0[0] * rr, d0[1] * rr), pk2(d0[2] * rr, d0[3] * rr), pk2(d1[0] * rr, d1[1] * rr), pk2(d1[2] * rr, d1[3] * rr)};
      stc16(rws, lane * 16, WS_PQ + ((rt * 8 + wr * 4 + mb) * 32 + head) * 1024, o);
    } }
  BAR();
  c_signal<true>(P.ws, 64 * l + C_O + (rt >> 1), lds, 5 * l + 1, 4);
  STAMP_END(2 + 5 * l);
}

constexpr int K2_NT = 16, K2_STAGE = 24576, K2_NS = 6;
template <bool P1B> DI void p2_load(const Params& P, int l, LAS char* lds, int j) {
  const int lane = fresh_lane();
  const int bid = fresh_s(blockIdx.x), xcd = bid & 7, loc = bid >> 3;
  const int rt = (xcd & 3) * 4 + (loc & 3), ct = (xcd >> 2) * 8 + (loc >> 2);
  const int rot = ROT_K2 ? ((loc >> 2) + 2 * (loc & 3)) & 7 : 0;
  const rsrc_t rA = mk_rsrc(P.ws, WS_BYTES), rW = mk_rsrc(P.wo, D_ * D_ * 2);
  const int r = lane >> 3, sl8 = lane & 7, f = ((r >> 1) & 1) | ((j & 1) << 1);
  const unsigned voffW = r * 2048 + ((sl8 ^ (2 * f)) * 16), voffL = lane * 16;
  auto stX = [&](int t, int sl) {
    const unsigned sa = WS_PQ + (unsigned)((rt * 8 + 2 * j) * 32768 + ((t & ~7) | ((t + rot) & 7)) * 2048);
#pragma unroll
    for (int u = 0; u < 4; ++u) dma16c(rA, lds + sl * K2_STAGE + (4 * j + u) * 1024, voffL, sa + (u >> 1) * 32768 + (u & 1) * 1024);
  };
  auto stW = [&](int t, int sl) {
    const unsigned sw = (unsigned)((((t & ~7) | ((t + rot) & 7)) * 64 + j * 8) * 2048 + ct * 128);
#pragma unroll
    for (int u = 0; u < 2; ++u) dma16(rW, lds + sl * K2_STAGE + 16384 + (j + 4 * u) * 1024, voffW, sw + u * 32 * 2048);
  };
#pragma unroll
  for (int t = 0; t < 5; ++t) stW(t, t);
  l_wait(P.ws, 64 * l + C_O + (rt >> 1), 32, lds, 5 * l + 2, j);
#pragma unroll
  for (int t = 0; t < 5; ++t) stX(t, t);
  WAITV(16); BAR(); stX(5, 5); stW(5, 5);
  WAITV(18); BAR(); stX(6, 0); stW(6, 0);
  WAITV(20); BAR(); stX(7, 1); stW(7, 1);
  WAITV(22); BAR(); stX(8, 2); stW(8, 2);
  int sl = 3;
  for (int t = 4; t < K2_NT - 5; ++t) {
    WAITV(24); BAR();
    stX(t + 5, sl); stW(t + 5, sl);
    if (PF_GU && l == 0) pf_lines<2>((const char*)((j & 1) ? P.wu : P.wg) + ((bid * 2 + (j >> 1)) * 112 + 10 * PF_GU + (t - 4) * 2) * 128);
    sl = sl == K2_NS - 1 ? 0 : sl + 1;
  }
  drain_bar<4, 6>();
  BAR();
}
DI void p2_comp(const Params& P, int l, LAS char* lds, int wid) {
  STAMP_DECL STAMP_AT(0);
  if (wid >= 4) {
#pragma unroll 1
    for (int i = 0; i < K2_NT + 1; ++i) BAR();
    return;
  }
  const int lane = fresh_lane();
  const int bid = fresh_s(blockIdx.x), xcd = bid & 7, loc = bid >> 3;
  const int rt = (xcd & 3) * 4 + (loc & 3), ct = (xcd >> 2) * 8 + (loc >> 2);
  const int m = lane & 15, g = lane >> 4, q = (lane >> 2) & 3, p = lane & 3;
  const int row0 = rt * 128 + wid * 32 + m;
  const unsigned off0 = (unsigned)(row0 * D_ + ct * 64 + 8 * g) * 2;
  int wa[4];
  { const int f = ((q >> 1) & 1) | ((g & 1) << 1);
#pragma unroll
    for (int nb = 0; nb < 4; ++nb) wa[nb] = 16384 + (8 * g + q) * 128 + (((4 * (nb >> 1) + p) ^ (2 * f)) * 16) + (((nb & 1) ^ (p >> 1)) * 8); }
  const int xa = wid * 4096 + lane * 16;
  f32x4 acc[2][4];
  { const brs_t rxi = mk_brs(l ? P.out : P.x0, S_ * D_ * 2);
    u32x4 res[2][2];
#pragma unroll
    for (int mb = 0; mb < 2; ++mb) { res[mb][0] = ldc16(rxi, off0, mb * 32768); res[mb][1] = ldc16(rxi, off0, mb * 32768 + 64); }
#pragma unroll
    for (int mb = 0; mb < 2; ++mb) res_unpack(acc[mb], res[mb], g >= 2, false); }
  bf16x8 xf[2][2][2], wf[2][2][4];
  auto fetch = [&](int set, int sl) {
    LAS char* b = lds + sl * K2_STAGE;
#pragma unroll
    for (int s = 0; s < 2; ++s) {
      xf[set][s][0] = ld128(b + xa + s * 1024); xf[set][s][1] = ld128(b + xa + s * 1024 + 2048);
#pragma unroll
      for (int nb = 0; nb < 4; ++nb) wf[set][s][nb] = ldtr(b + wa[nb] + s * 4096, 512);
    }
  };
  auto mma = [&](int set) {
#pragma unroll
    for (int s = 0; s < 2; ++s)
#pragma unroll
      for (int nb = 0; nb < 4; ++nb)
#pragma unroll
        for (int mb = 0; mb < 2; ++mb) acc[mb][nb] = mfma(wf[set][s][nb], xf[set][s][mb], acc[mb][nb]);
  };
#define K2_MIX() do { _Pragma("unroll") for (int i_ = 0; i_ < 10; ++i_) { SGB(0x8, 1); SGB(0x100, 2); } SGB(0x8, 6); SB0(); } while (0)
  BAR();
  STAMP_AT(1);
  fetch(0, 0);
  SB0();
  int sl = 1;
  for (int t = 1; t < K2_NT - 1; t += 2) {
    WAITL0(); BAR();
    fetch(1, sl); mma(0); K2_MIX();
    sl = sl == K2_NS - 1 ? 0 : sl + 1;
    WAITL0(); BAR();
    fetch(0, sl); mma(1); K2_MIX();
    sl = sl == K2_NS - 1 ? 0 : sl + 1;
  }
  WAITL0(); BAR();
  fetch(1, sl); mma(0); K2_MIX();
  mma(1);
  STAMP_AT(2);
  const brs_t rws = mk_brs(P.ws, WS_BYTES);
#pragma unroll
  for (int mb = 0; mb < 2; ++mb)
    x_store(acc[mb], rws, WS_X1 + off0 + xs_adj(m) + mb * 32768, rws, WS_ST + (unsigned)(2 * l + 1) * 262144u + (ct * 2048 + row0 + mb * 16) * 8, m, g, true);
  BAR();
  c_signal<true>(P.ws, 64 * l + C_X1 + (rt >> 1), lds, 5 * l + 2, 4);
  STAMP_END(3 + 5 * l);
}

DI void p3_load(const Params& P, int l, LAS char* lds, int j) {
  const int lane = fresh_lane(), tid = j * 64 + lane;
  const int bid = fresh_s(blockIdx.x), xcd = bid & 7, loc = bid >> 3;
  const int rt = (xcd & 1) * 4 + (loc & 3), ct = (xcd >> 1) * 8 + (loc >> 2);
  const int rot = ROT_K3 ? ((loc >> 2) + 2 * (loc & 3)) & 7 : 0;
  const int mt = j & 1, hi2 = j >> 1;
  unsigned voffA, voffB;
  { const int r = lane >> 3, sl8 = lane & 7; voffA = r * 2048 + ((sl8 ^ (r & 6)) * 16);
    const int qq = lane >> 4, sl = lane & 15, f = qq | (hi2 << 2), ch = sl ^ (2 * f) ^ mt; voffB = ch < 14 ? (unsigned)(qq * 7168 + ch * 16) : OOB_V; }
  const rsrc_t rS = mk_rsrc(P.ws, WS_BYTES), rW = mk_rsrc(mt ? P.wu : P.wg, D_ * F_ * 2);
  auto stA = [&](int half, int t) {
    const unsigned sb = WS_X1 + (unsigned)((rt * 256 + half * 128 + j * 32) * 2048 + ((t & 8) | ((t + rot) & 7)) * 128);
#pragma unroll
    for (int u = 0; u < 4; ++u) dma16c(rS, lds + half * 32768 + (t & 1) * 16384 + (4 * j + u) * 1024, voffA, sb + u * 16384);
  };
  auto stB = [&](int s, int t) {
    const unsigned sb = (unsigned)((((t & 8) | ((t + rot) & 7)) * 64 + s * 32 + hi2 * 8) * 7168 + ct * 224);
#pragma unroll
    for (int u = 0; u < 4; ++u) { const int i = (u & 1) + 4 * (u >> 1);
      dma16(rW, lds + 65536 + ((t & 1) * 2 + s) * 16384 + mt * 8192 + (i + 2 * hi2) * 1024, voffB, sb + i * 4 * 7168); }
  };
  stB(0, 0); stB(1, 0); stB(0, 1);
  l_wait(P.ws, 64 * l + C_X1 + rt, 32, lds, 5 * l + 3, j);
  stA(0, 0); stA(1, 0); stA(0, 1);
  u32x2 sv[16]; f32x4 csv;
  { const int t = tid;
    const unsigned so = WS_ST + (unsigned)(2 * l + 1) * 262144u + rt * 2048, vo = t * 8;
#pragma unroll
    for (int i = 0; i < 16; ++i) ALOAD8C(sv[i], rS, vo, so + i * 16384);
    const int ci = t < 224 ? t : 0;
    ALOAD16(csv, rS, (ci + (ci >= 112 ? F_ - 112 : 0)) * 16, WS_CS + (3072 + ct * 112) * 16); }
  WAITV(25); BAR();
  stA(1, 1); WAITV(25); BAR();
  stB(1, 1); WAITV(33); BAR();
  stB(0, 2); WAITV(37); BAR();
  stA(0, 2); WAITV(16); BAR();
  stA(1, 2);
  { const int t = tid;
#pragma unroll
    for (int i = 0; i < 16; ++i) PIN(sv[i]);
    PIN(csv);
    *(LAS f32x2*)(lds + L_STATS + t * 8) = combine_stats<16, false>(sv);
    if (t < 224) *(LAS float*)(lds + L_CS + t * 4) = (csv[0] + csv[1]) + (csv[2] + csv[3]);
    WAITL0(); }
  WAITV(16); BAR();
  stB(1, 2); WAITV(16); BAR();
  stB(0, 3); WAITV(16); BAR();
  stA(0, 3); WAITV(16); BAR();
  for (int t = 2; t < 14; ++t) {
    const char* pfp = (const char*)P.wd + ((bid * 4 + j) * 56 + (t - 2) * 7) * 128;
    const bool pf = l == 0 && t < 10;
    stA(1, t + 1); if (pf) pf_lines<2>(pfp); WAITV(16); BAR();
    stB(1, t + 1); if (pf) pf_lines<2>(pfp + 256); WAITV(16); BAR();
    stB(0, t + 2); if (pf) pf_lines<2>(pfp + 512); WAITV(16); BAR();
    stA(0, t + 2); if (pf) pf_lines<1>(pfp + 768); WAITV(16); BAR();
  }
  stA(1, 15); WAITV(16); BAR();
  stB(1, 15); WAITV(16); BAR();
  drain_bar<3, 4>();
  BAR(); BAR();
  BAR();
}
DI void p3_comp(const Params& P, int l, LAS char* lds, int wid) {
  STAMP_DECL STAMP_AT(0);
  const int bid = fresh_s(blockIdx.x), xcd = bid & 7, loc = bid >> 3;
  const int rt = (xcd & 1) * 4 + (loc & 3), ct = (xcd >> 1) * 8 + (loc >> 2);
  const int grp = wid >> 2, wrl = (wid >> 1) & 1, wc = wid & 1;
  f32x4 acc[4][7];
  {
    const int lane = fresh_lane();
    const int m = lane & 15, g = lane >> 4, q = (lane >> 2) & 3, p = lane & 3;
    int xa[2], wa[7];
#pragma unroll
    for (int s = 0; s < 2; ++s) xa[s] = grp * 32768 + (wrl * 64 + m) * 128 + (((4 * s + g) ^ (m & 6)) * 16);
    { const int f2 = 2 * (q | ((g & 1) << 2)), ro = 65536 + (8 * g + q) * 256, c0 = 7 * wc;
#pragma unroll
      for (int mm = 0; mm < 2; ++mm) {
#pragma unroll
        for (int b = 0; b < 2; ++b) wa[3 * mm + b] = ro + mm * 8192 + (((c0 + p) ^ f2 ^ mm) * 16) + ((b ^ (p >> 1)) * 8);
        wa[3 * mm + 2] = ro + mm * 8192 + (((c0 + 4 + (p >> 1)) ^ f2 ^ mm) * 16) + (p & 1) * 8;
      }
      wa[6] = ro + (p >> 1) * 8192 + (((c0 + 6) ^ f2 ^ (p >> 1)) * 16) + (p & 1) * 8; }
#pragma unroll
    for (int a = 0; a < 4; ++a)
#pragma unroll
      for (int b = 0; b < 7; ++b) acc[a][b] = (f32x4){0.f, 0.f, 0.f, 0.f};
    BAR();
    STAMP_AT(1);
    if (grp == 1) BAR();
    for (int t2 = 0; t2 < 16; t2 += 2) {
#pragma unroll
      for (int buf = 0; buf < 2; ++buf) {
#pragma unroll
        for (int s = 0; s < 2; ++s) {
          bf16x8 xf[4], wf[7];
#pragma unroll
          for (int mb = 0; mb < 4; ++mb) xf[mb] = ld128(lds + xa[s] + buf * 16384 + mb * 2048);
#pragma unroll
          for (int T = 0; T < 7; ++T) wf[T] = ldtr(lds + wa[T] + (buf * 2 + s) * 16384, 1024);
          WAITL0();
          BAR();
          __builtin_amdgcn_s_setprio(1);
#pragma unroll
          for (int mb = 0; mb < 4; ++mb)
#pragma unroll
            for (int T0 = 0; T0 < 7; ++T0) { const int T = (SNAKE && (mb & 1)) ? 6 - T0 : T0; acc[mb][T] = mfma(wf[T], xf[mb], acc[mb][T]); }
          __builtin_amdgcn_s_setprio(0);
          if (buf == 0 || s == 0 || t2 < 14 || grp == 0) BAR();
        }
      }
    }
  }
  STAMP_AT(2);
  const int tz = fresh_lane();
  const int m = tz & 15, g = tz >> 4;
  const LAS f32x2* stt = (const LAS f32x2*)(lds + L_STATS);
  const LAS float* csl = (const LAS float*)(lds + L_CS);
  f32x4 c4[7];
#pragma unroll
  for (int mm = 0; mm < 2; ++mm) {
#pragma unroll
    for (int b = 0; b < 2; ++b) c4[3 * mm + b] = *(const LAS f32x4*)(csl + mm * 112 + wc * 56 + 8 * g + 4 * (b ^ (g >> 1)));
    c4[3 * mm + 2] = *(const LAS f32x4*)(csl + mm * 112 + wc * 56 + 32 + 4 * g);
  }
  c4[6] = *(const LAS f32x4*)(csl + (g >> 1) * 112 + wc * 56 + 48 + 4 * (g & 1));
  const bool hi = g >= 2;
  const brs_t rws = mk_brs(P.ws, WS_BYTES);
  const unsigned hb = WS_H + (unsigned)(rt * 16 + grp * 8 + wrl * 4) * 114688u + (unsigned)(7 * (2 * ct + wc)) * 256u;
  const unsigned v16 = m * 16 + g * 256, v8 = m * 16 + (g >> 1) * 256 + (g & 1) * 114688, vz = m * 16 + ((g >> 1) + 2 * (g & 1)) * 114688;
  f32x4 zv[4];
  u32x2 h2[4];
#pragma unroll
  for (int mb = 0; mb < 4; ++mb) {
    const f32x2 mr = stt[grp * 128 + wrl * 64 + mb * 16 + m];
    const float r2 = mr[1] * mr[1];
    u32x2 hv[3];
#pragma unroll
    for (int b = 0; b < 3; ++b) {
      const f32x4 hh = ((acc[mb][b] - mr[0] * c4[b]) * r2) * (acc[mb][3 + b] - mr[0] * c4[3 + b]);
      hv[b][0] = pk2(hh[0], hh[1]); hv[b][1] = pk2(hh[2], hh[3]);
    }
    zv[mb] = (acc[mb][6] - mr[0] * c4[6]) * mr[1];
    const u32x4 o = {hi ? hv[1][0] : hv[0][0], hi ? hv[1][1] : hv[0][1], hi ? hv[0][0] : hv[1][0], hi ? hv[0][1] : hv[1][1]};
    stc16(rws, v16, hb + mb * 114688, o);
    h2[mb] = hv[2];
    if (mb & 1) {
      const auto s0 = __builtin_amdgcn_permlane16_swap(h2[mb - 1][0], h2[mb][0], false, false), s1 = __builtin_amdgcn_permlane16_swap(h2[mb - 1][1], h2[mb][1], false, false);
      const u32x4 o2 = {s0[0], s1[0], s0[1], s1[1]};
      stc16(rws, v8, hb + (mb - 1) * 114688 + 1024, o2);
    }
  }
  { u32x2 oz[2];
#pragma unroll
    for (int mp = 0; mp < 2; ++mp) {
      float hh[4];
#pragma unroll
      for (int e = 0; e < 4; ++e) {
        const auto r = __builtin_amdgcn_permlane32_swap(__float_as_uint(zv[2 * mp][e]), __float_as_uint(zv[2 * mp + 1][e]), false, false);
        hh[e] = __uint_as_float(r[0]) * __uint_as_float(r[1]);
      }
      oz[mp][0] = pk2(hh[0], hh[1]); oz[mp][1] = pk2(hh[2], hh[3]);
    }
    const auto s0 = __builtin_amdgcn_permlane16_swap(oz[0][0], oz[1][0], false, false), s1 = __builtin_amdgcn_permlane16_swap(oz[0][1], oz[1][1], false, false);
    const u32x4 o2 = {s0[0], s1[0], s0[1], s1[1]};
    stc16(rws, vz, hb + 1536, o2); }
  BAR();
  c_signal<true>(P.ws, 64 * l + C_H + rt * 2 + (ct >> 4), lds, 5 * l + 3, 8);
  STAMP_END(4 + 5 * l);
}

constexpr int K4_NT = 28, K4_STAGE = 32768, K4_NS = 4;
DI void p4_load(const Params& P, int l, LAS char* lds, int j) {
  const int lane = fresh_lane();
  const int bid = fresh_s(blockIdx.x), xcd = bid & 7, loc = bid >> 3;
  const int kh = xcd & 1, rt = (xcd >> 1) * 4 + (loc & 3), ct = loc >> 2;
  const int rot = ROT_K4 ? (ct + 2 * (loc & 3)) % 7 : 0;
  const rsrc_t rA = mk_rsrc(P.ws, WS_BYTES), rW = mk_rsrc(P.wd, F_ * D_ * 2);
  const int r = lane >> 3, sl8 = lane & 7, f = ((r >> 1) & 1) | ((j & 1) << 1);
  const unsigned voffW = r * 2048 + ((sl8 ^ (2 * f)) * 16), voffL = lane * 16;
  auto stX = [&](int kt, int sl) {
    const unsigned sa = WS_H + (unsigned)((rt * 8 + 2 * j) * 114688 + kt * 2048);
#pragma unroll
    for (int u = 0; u < 4; ++u) dma16c(rA, lds + sl * K4_STAGE + (4 * j + u) * 1024, voffL, sa + (u >> 1) * 114688 + (u & 1) * 1024);
  };
  auto stW = [&](int kt, int sl) {
    const unsigned sw = (unsigned)((kt * 64 + j * 8) * 2048 + ct * 256);
#pragma unroll
    for (int u = 0; u < 4; ++u) dma16(rW, lds + sl * K4_STAGE + 16384 + (u >> 1) * 8192 + (j + 4 * (u & 1)) * 1024, voffW, sw + (u >> 1) * 128 + (u & 1) * 32 * 2048);
  };
  const int k0 = kh * K4_NT + rot, k1 = kh * K4_NT + (rot + 1) % 7, k2 = kh * K4_NT + (rot + 2) % 7;
  stW(k0, 0); stW(k1, 1); stW(k2, 2);
  l_wait(P.ws, 64 * l + C_H + (rt >> 1) * 2 + kh, 16, lds, 5 * l + 4, j);
  stX(k0, 0); stX(k1, 1); stX(k2, 2);
  int w0 = kh * K4_NT, wr_ = (rot + 3) % 7, sl = 3;
  auto next = [&]() { const int kt = w0 + wr_; stX(kt, sl); stW(kt, sl); wr_ = wr_ == 6 ? 0 : wr_ + 1; if (wr_ == rot) w0 += 7; sl = (sl + 1) & 3; };
  WAITV(8); BAR(); next();
  WAITV(12); BAR(); next();
  for (int t = 2; t < K4_NT - 3; ++t) { WAITV(16); BAR(); next(); }
  drain_bar<2, 8>();
  BAR();
}
DI void p4_comp(const Params& P, int l, LAS char* lds, int wid) {
  STAMP_DECL STAMP_AT(0);
  if (wid >= 4) {
#pragma unroll 1
    for (int i = 0; i < K4_NT + 1; ++i) BAR();
    return;
  }
  const int lane = fresh_lane();
  const int bid = fresh_s(blockIdx.x), xcd = bid & 7, loc = bid >> 3;
  const int kh = xcd & 1, rt = (xcd >> 1) * 4 + (loc & 3), ct = loc >> 2;
  const int wr = wid >> 1, wc = wid & 1;
  const int m = lane & 15, g = lane >> 4, q = (lane >> 2) & 3, p = lane & 3;
  const int row0 = rt * 128 + wr * 64 + kh * 32 + m;
  const unsigned off0 = (unsigned)(row0 * D_ + ct * 128 + wc * 64 + 8 * g) * 2;
  int wa[4];
  { const int f = ((q >> 1) & 1) | ((g & 1) << 1);
#pragma unroll
    for (int nb = 0; nb < 4; ++nb) wa[nb] = 16384 + wc * 8192 + (8 * g + q) * 128 + (((4 * (nb >> 1) + p) ^ (2 * f)) * 16) + (((nb & 1) ^ (p >> 1)) * 8); }
  const int xa0 = wr * 8192 + lane * 16 + kh * 4096, xa1 = wr * 8192 + lane * 16 + 4096 - kh * 4096;
  f32x4 acc[4][4];
#pragma unroll
  for (int a = 0; a < 4; ++a)
#pragma unroll
    for (int b = 0; b < 4; ++b) acc[a][b] = (f32x4){0.f, 0.f, 0.f, 0.f};
  bf16x8 xf[2][4], wf[2][4];
  auto fetch = [&](int s, int sl) {
    LAS char* b = lds + sl * K4_STAGE;
    xf[s][0] = ld128(b + xa0 + s * 1024); xf[s][1] = ld128(b + xa0 + s * 1024 + 2048);
    xf[s][2] = ld128(b + xa1 + s * 1024); xf[s][3] = ld128(b + xa1 + s * 1024 + 2048);
#pragma unroll
    for (int nb = 0; nb < 4; ++nb) wf[s][nb] = ldtr(b + wa[nb] + s * 4096, 512);
  };
  auto mma = [&](int s) {
#pragma unroll
    for (int nb = 0; nb < 4; ++nb)
#pragma unroll
      for (int mb = 0; mb < 4; ++mb) acc[mb][nb] = mfma(wf[s][nb], xf[s][mb], acc[mb][nb]);
  };
#define K4_MIX() do { _Pragma("unroll") for (int i_ = 0; i_ < 12; ++i_) { SGB(0x8, 1); SGB(0x100, 1); } SGB(0x8, 4); SB0(); } while (0)
  const brs_t rws = mk_brs(P.ws, WS_BYTES);
  BAR();
  STAMP_AT(1);
  fetch(0, 0);
  SB0();
  int sl = 0;
  for (int t = 0; t < K4_NT - 1; ++t) {
    fetch(1, sl); mma(0); K4_MIX();
    sl = (sl + 1) & (K4_NS - 1);
    WAITL0(); BAR();
    fetch(0, sl); mma(1); K4_MIX();
  }
  fetch(1, sl); mma(0); K4_MIX();
  mma(1);
  STAMP_AT(2);
  const int pair = rt * 8 + ct;
  u32x4 res[2][2];
#pragma unroll
  for (int mb = 0; mb < 2; ++mb) { res[mb][0] = ldc16(rws, off0, WS_X1 + mb * 32768); res[mb][1] = ldc16(rws, off0, WS_X1 + mb * 32768 + 64); }
  FENCE();
#pragma unroll
  for (int i = 0; i < 4; ++i) { const f32x4 a = acc[2 + (i >> 1)][2 * (i & 1)], b = acc[2 + (i >> 1)][2 * (i & 1) + 1];
    const u32x4 o = {pk2(a[0], a[1]), pk2(a[2], a[3]), pk2(b[0], b[1]), pk2(b[2], b[3])};
    stc16(rws, lane * 16, WS_XCH + pair * 65536 + ((kh ^ 1) * 4 + wid) * 8192 + i * 1024, o); }
#pragma unroll
  for (int mb = 0; mb < 2; ++mb) { res_unpack(acc[mb], res[mb], g >= 2, true); PIN(acc[mb][0]); PIN(acc[mb][1]); PIN(acc[mb][2]); PIN(acc[mb][3]); }
  WAITV(0);
  volatile LAS unsigned* relay = (volatile LAS unsigned*)(lds + L_SYNC + 48);
  { unsigned old = 0;
    if (lane == 0) old = __hip_atomic_fetch_add((LAS unsigned*)(lds + L_SYNC + 52), 1u, __ATOMIC_RELAXED, __HIP_MEMORY_SCOPE_WORKGROUP);
    old = __builtin_amdgcn_readfirstlane(old);
    if ((old & 3) == 3) {
      if (lane == 0) __hip_atomic_store(cnt_ptr(P.ws, C_FLAG + 256 * l + pair * 2 + kh), 1u, __ATOMIC_RELAXED, __HIP_MEMORY_SCOPE_AGENT);
      poll_ge(cnt_ptr(P.ws, C_FLAG + 256 * l + pair * 2 + (kh ^ 1)), 1);
      *relay = l + 1;
    } else { unsigned spins = 0; while ((unsigned)__builtin_amdgcn_readfirstlane(*relay) < (unsigned)(l + 1) && ++spins < (1u << 24)) __builtin_amdgcn_s_sleep(1); }
    FENCE(); }
  u32x4 in[8];
#pragma unroll
  for (int i = 0; i < 4; ++i) in[i] = ldc16(rws, lane * 16, WS_XCH + pair * 65536 + (kh * 4 + wid) * 8192 + i * 1024);
  BAR();
#pragma unroll
  for (int i = 0; i < 4; ++i) { const f32x4 a = {bflo(in[i][0]), bfhi(in[i][0]), bflo(in[i][1]), bfhi(in[i][1])}, b = {bflo(in[i][2]), bfhi(in[i][2]), bflo(in[i][3]), bfhi(in[i][3])};
    acc[i >> 1][2 * (i & 1)] += a; acc[i >> 1][2 * (i & 1) + 1] += b; }
  const brs_t rxo = mk_brs(P.out, S_ * D_ * 2);
#pragma unroll
  for (int mb = 0; mb < 2; ++mb)
    x_store(acc[mb], rxo, off0 + xs_adj(m) + mb * 32768, rws, WS_ST + (unsigned)((2 * l + 2) & 3) * 262144u + ((ct * 2 + wc) * 2048 + row0 + mb * 16) * 8, m, g, l == 0);
  if (l == 0) c_signal<true>(P.ws, 64 + C_X + rt, lds, 4, 4);
  STAMP_END(5 + 5 * l);
}

DI void init_sync(LAS char* lds) { if (threadIdx.x < 32) *(LAS unsigned*)(lds + L_SYNC + threadIdx.x * 4) = 0u; WAITL0(); }

__global__ __launch_bounds__(768, 3) void k_mega(Params P) {
  extern __shared__ __attribute__((aligned(16))) char smem[];
  LAS char* lds = (LAS char*)smem;
  const int wid = __builtin_amdgcn_readfirstlane(threadIdx.x >> 6);
  if (wid >= 8) {
    const int j = wid & 3;
#pragma unroll 1
    for (int l = 0; l < 2; ++l) { p1_load(P, fresh_s(l), lds, fresh_s(j)); p2_load<true>(P, fresh_s(l), lds, fresh_s(j)); p3_load(P, fresh_s(l), lds, fresh_s(j)); p4_load(P, fresh_s(l), lds, fresh_s(j)); }
  } else {
    init_sync(lds);
#pragma unroll 1
    for (int l = 0; l < 2; ++l) {
      p1_comp(P, fresh_s(l), lds, fresh_s(wid)); p2_comp(P, fresh_s(l), lds, fresh_s(wid));
      p3_comp(P, fresh_s(l), lds, fresh_s(wid)); p4_comp(P, fresh_s(l), lds, fresh_s(wid));
    }
  }
}

extern "C" void kernel_launch(void* const* d_in, const int* in_sizes, int n_in, void* d_out, int out_size, void* d_ws, size_t ws_size, hipStream_t stream) {
  Params P;
  P.x0 = (const u16*)d_in[0]; P.wqkv = (const u16*)d_in[1]; P.wo = (const u16*)d_in[2];
  P.wg = (const u16*)d_in[4]; P.wu = (const u16*)d_in[5]; P.wd = (const u16*)d_in[6];
  P.out = (u16*)d_out; P.ws = (char*)d_ws;
  k0_prep<<<K0_GRID, 256, 0, stream>>>(P);
  (void)hipFuncSetAttribute((const void*)k_mega, hipFuncAttributeMaxDynamicSharedMemorySize, L_BYTES);
  k_mega<<<256, 768, L_BYTES, stream>>>(P);
}
```

```cpp
#include <hip/hip_runtime.h>
#include <hip/hip_bf16.h>
#include <stdint.h>

#define LAS __attribute__((address_space(3)))
#define DI __device__ __forceinline__
typedef unsigned short u16;
typedef __bf16 bf16x8 __attribute__((ext_vector_type(8)));
typedef __bf16 bf16x2 __attribute__((ext_vector_type(2)));
typedef short s16x4 __attribute__((ext_vector_type(4)));
typedef float f32x2 __attribute__((ext_vector_type(2)));
typedef float f32x4 __attribute__((ext_vector_type(4)));
typedef unsigned u32x2 __attribute__((ext_vector_type(2)));
typedef unsigned u32x4 __attribute__((ext_vector_type(4)));
typedef u32x4 rsrc_t;
typedef __amdgpu_buffer_rsrc_t brs_t;

constexpr int S_ = 2048, D_ = 1024, F_ = 3584;
#define MEGA 1
#define QDEF 7
#define SNAKE 1
#define P4_LATEBAR 1
#define HELP_PRIO 2
#define X_FULL 1
#define PF_ON 1
#define POLL_SLEEP 1
#define ROT_K2 0
#define ROT_K4 1
constexpr unsigned OOB_V = 0x80000000u;
constexpr unsigned WS_PQ = 0, WS_KVP = 4u << 20, WS_X1 = 8u << 20, WS_H = 12u << 20, WS_XCH = 27u << 20, WS_ST = 35u << 20, WS_CS = 36u << 20, WS_CNT = 37u << 20,
                   WS_BYTES = 38u << 20;
#define NREP 8
constexpr int C_X = 0, C_KV = 16, C_O = 32, C_X1 = 40, C_H = 48, C_FLAG = 128 * NREP, C_N = C_FLAG + 512;
constexpr unsigned WS_TAG = WS_CNT + C_N * 128, T_X0 = 0, T_CS = 16, WS_EPOCH = WS_TAG + 32 * 128;
static_assert(C_N == 6 * 256 && NREP <= 16, "counter zeroing map");
constexpr int L_STATS = 147456, L_CS = L_STATS + 2048, L_CS1 = L_CS + 1024, L_KV = L_CS1 + 1024, L_SYNC = L_KV + 4224, L_BYTES = L_SYNC + 128;

struct Params { const u16* x0; const u16* wqkv; const u16* wo; const u16* wg; const u16* wu; const u16* wd; u16* out; char* ws; };

typedef const __attribute__((address_space(4))) Params* KP;
DI Params fresh_params() {
  KP p = (KP)__builtin_amdgcn_kernarg_segment_ptr(); asm volatile("" : "+s"(p));
  Params r; r.x0 = p->x0; r.wqkv = p->wqkv; r.wo = p->wo; r.wg = p->wg; r.wu = p->wu; r.wd = p->wd; r.out = p->out; r.ws = p->ws; return r;
}
DI rsrc_t mk_rsrc(const void* p, unsigned bytes) {
  const unsigned long long a = (unsigned long long)p;
  rsrc_t r = {(unsigned)a, (unsigned)(a >> 32) & 0xffffu, bytes, 0x00020000u};
  return r;
}
DI brs_t mk_brs(const void* p, unsigned bytes) { return __builtin_amdgcn_make_buffer_rsrc((void*)p, 0, bytes, 0x00020000); }
DI void dma16(rsrc_t r, LAS char* dst, unsigned voff, unsigned soff) {
  asm volatile("s_mov_b32 m0, %0\n\ts_nop 0\n\tbuffer_load_dwordx4 %1, %2, %3 offen lds" :: "s"((unsigned)(size_t)dst), "v"(voff), "s"(r), "s"(soff) : "memory");
}
DI void dma16c(rsrc_t r, LAS char* dst, unsigned voff, unsigned soff) {
  asm volatile("s_mov_b32 m0, %0\n\ts_nop 0\n\tbuffer_load_dwordx4 %1, %2, %3 offen sc1 lds" :: "s"((unsigned)(size_t)dst), "v"(voff), "s"(r), "s"(soff) : "memory");
}
DI u32x4 ldc16(brs_t r, unsigned voff, unsigned soff) { return __builtin_amdgcn_raw_buffer_load_b128(r, voff, soff, 16); }
DI void stc16(brs_t r, unsigned voff, unsigned soff, u32x4 v) { __builtin_amdgcn_raw_buffer_store_b128(v, r, voff, soff, 16); }
DI void stc16(brs_t r, unsigned voff, unsigned soff, f32x4 v) { stc16(r, voff, soff, __builtin_bit_cast(u32x4, v)); }
DI void stc8(brs_t r, unsigned voff, unsigned soff, u32x2 v) { __builtin_amdgcn_raw_buffer_store_b64(v, r, voff, soff, 16); }
DI f32x4 mfma(bf16x8 a, bf16x8 b, f32x4 c) { return __builtin_amdgcn_mfma_f32_16x16x32_bf16(a, b, c, 0, 0, 0); }
DI bf16x8 ld128(LAS char* p) { return *(LAS bf16x8*)p; }
DI bf16x8 ldtr(LAS char* p, int off2) {
  s16x4 a = __builtin_amdgcn_ds_read_tr16_b64_v4i16((LAS s16x4*)p);
  s16x4 b = __builtin_amdgcn_ds_read_tr16_b64_v4i16((LAS s16x4*)(p + off2));
  return __builtin_bit_cast(bf16x8, __builtin_shufflevector(a, b, 0, 1, 2, 3, 4, 5, 6, 7));
}
DI unsigned pk2(float a, float b) { f32x2 v = {a, b}; return __builtin_bit_cast(unsigned, __builtin_convertvector(v, bf16x2)); }
DI float bflo(unsigned u) { return __uint_as_float(u << 16); }
DI float bfhi(unsigned u) { return __uint_as_float(u & 0xffff0000u); }
DI float phi_ln(float u, float rs, float rs2) {
  float e; asm("v_exp_f32 %0, %1 clamp" : "=v"(e) : "v"(u * rs2));
  return fmaxf(__builtin_fmaf(u, rs, 1e-6f), 1e-6f) + e;
}
DI float xsum(float s) {
  const auto a = __builtin_amdgcn_permlane16_swap(__float_as_uint(s), __float_as_uint(s), false, false);
  s = __uint_as_float(a[0]) + __uint_as_float(a[1]);
  const auto b = __builtin_amdgcn_permlane32_swap(__float_as_uint(s), __float_as_uint(s), false, false);
  return __uint_as_float(b[0]) + __uint_as_float(b[1]);
}
#define SGB(mask, n) __builtin_amdgcn_sched_group_barrier(mask, n, 0)
#define SB0() __builtin_amdgcn_sched_barrier(0)
#define PIN(x) asm volatile("" : "+v"(x))
#define FENCE() asm volatile("" ::: "memory")
#define BAR() do { FENCE(); __builtin_amdgcn_s_barrier(); FENCE(); __builtin_amdgcn_sched_barrier(0); } while (0)
#define WAITV(n) asm volatile("s_waitcnt vmcnt(%0)" :: "i"(n) : "memory")
#define WAITL0() do { FENCE(); __builtin_amdgcn_s_waitcnt(0xc07f); FENCE(); } while (0)
DI int fresh_s(int x) { asm volatile("" : "+s"(x)); return x; }
DI int fresh_lane() { int t; asm volatile("v_mbcnt_lo_u32_b32 %0, -1, 0\n\tv_mbcnt_hi_u32_b32 %0, -1, %0" : "=v"(t)); return t; }

#define STAMP_DECL
#define STAMP_AT(i)
#define STAMP_END(sl_)

template <int N> DI void pf_lines(const void* p) {
#pragma unroll
  for (int i = 0; i < 2 * N; ++i) asm volatile("s_load_dword s100, %0, %1" :: "s"(p), "i"(i * 64) : "s100");
}
DI unsigned* cnt_ptr(char* ws, int idx) { return (unsigned*)(ws + WS_CNT + idx * 128); }
DI unsigned* rep_ptr(char* ws, int idx) { return cnt_ptr(ws, idx * NREP + ((blockIdx.x >> 3) + (blockIdx.x & 7) * 5) % NREP); }
DI void poll_ge(unsigned* c, unsigned target) {
  unsigned spins = 0;
  while ((unsigned)__builtin_amdgcn_readfirstlane(__hip_atomic_load(c, __ATOMIC_RELAXED, __HIP_MEMORY_SCOPE_AGENT)) < target && ++spins < (1u << 21)) __builtin_amdgcn_s_sleep(POLL_SLEEP);
  FENCE();
}
DI void l_wait(char* ws, int idx, unsigned target, LAS char* lds, unsigned epoch, int j) {
  if (!MEGA) return;
  volatile LAS unsigned* relay = (volatile LAS unsigned*)(lds + L_SYNC);
  if (j == 0) { poll_ge(rep_ptr(ws, idx), target); *relay = epoch; }
  else { unsigned spins = 0; while ((unsigned)__builtin_amdgcn_readfirstlane(*relay) < epoch && ++spins < (1u << 24)) __builtin_amdgcn_s_sleep(1); }
  FENCE();
}
template <bool REP> DI void c_signal(char* ws, int idx, LAS char* lds, int arr, unsigned nw) {
  if (!MEGA) return;
  WAITV(0);
  const int lane = fresh_lane();
  unsigned old = 0;
  if (lane == 0) old = __hip_atomic_fetch_add((LAS unsigned*)(lds + L_SYNC + 4 + arr * 4), 1u, __ATOMIC_RELAXED, __HIP_MEMORY_SCOPE_WORKGROUP);
  if ((unsigned)__builtin_amdgcn_readfirstlane(old) == nw - 1 && lane < (REP ? NREP : 1))
    __hip_atomic_fetch_add(cnt_ptr(ws, REP ? idx * NREP + lane : idx), 1u, __ATOMIC_RELAXED, __HIP_MEMORY_SCOPE_AGENT);
}

typedef unsigned long long u64;
DI u64 call_tag(const char* ws) {
  const u64 e = __hip_atomic_load((const u64*)(ws + WS_EPOCH), __ATOMIC_RELAXED, __HIP_MEMORY_SCOPE_AGENT);
  u64 z = ((u64)(unsigned)__builtin_amdgcn_readfirstlane((unsigned)(e >> 32)) << 32 | (unsigned)__builtin_amdgcn_readfirstlane((unsigned)e)) + 0x9E3779B97F4A7C15ull;
  z = (z ^ (z >> 30)) * 0xBF58476D1CE4E5B9ull; z = (z ^ (z >> 27)) * 0x94D049BB133111EBull;
  return z ^ (z >> 31);
}
DI u64* tag_ptr(char* ws, int blk) { return (u64*)(ws + WS_TAG + blk * 128); }
DI void tag_poll(u64* f, int np, u64 tag) {
  const int lane = fresh_lane();
  unsigned spins = 0;
  while (!__all(__hip_atomic_load(f + (lane < np ? lane : 0), __ATOMIC_RELAXED, __HIP_MEMORY_SCOPE_AGENT) == tag) && ++spins < (1u << 21)) __builtin_amdgcn_s_sleep(2);
  FENCE();
}
template <int NT, bool PAIR> DI f32x2 combine_stats(const u32x2 (&u)[NT]) {
  float s = 0.f;
#pragma unroll
  for (int i = 0; i < NT; ++i) s += __uint_as_float(u[i][0]);
  if (PAIR) s += __shfl_xor(s, 1);
  const float mean = s * (1.f / 1024.f);
  float q = 0.f;
#pragma unroll
  for (int i = 0; i < NT; ++i) { const float d = __uint_as_float(u[i][0]) * (1.f / 64.f) - mean; q += __uint_as_float(u[i][1]) + 64.f * d * d; }
  if (PAIR) q += __shfl_xor(q, 1);
  f32x2 r; r[0] = mean; r[1] = __builtin_amdgcn_rsqf(q * (1.f / 1024.f) + 1e-5f);
  return r;
}
DI u32x4 ones_bf16() { unsigned o = 0x3f803f80u; asm volatile("" : "+v"(o)); const u32x4 r = {o, o, o, o}; return r; }
DI void res_unpack(f32x4 (&a)[4], const u32x4 (&res)[2], bool hi, bool add) {
#pragma unroll
  for (int pr = 0; pr < 2; ++pr) {
    const unsigned u0 = hi ? res[pr][2] : res[pr][0], u1 = hi ? res[pr][3] : res[pr][1], u2 = hi ? res[pr][0] : res[pr][2], u3 = hi ? res[pr][1] : res[pr][3];
    const f32x4 x = {bflo(u0), bfhi(u0), bflo(u1), bfhi(u1)}, y = {bflo(u2), bfhi(u2), bflo(u3), bfhi(u3)};
    if (add) { a[2 * pr] += x; a[2 * pr + 1] += y; } else { a[2 * pr] = x; a[2 * pr + 1] = y; }
  }
}
DI void x_store(const f32x4 (&a)[4], brs_t rxo, unsigned xoff, brs_t rws, unsigned stoff, int m, int g, bool stats) {
  const bool hi = g >= 2;
  const u32x4 ones4 = ones_bf16();
  f32x4 sm = {0.f, 0.f, 0.f, 0.f}, sq = {0.f, 0.f, 0.f, 0.f};
  u32x4 o[2];
#pragma unroll
  for (int pr = 0; pr < 2; ++pr) {
    const unsigned p0 = pk2(a[2 * pr][0], a[2 * pr][1]), p1 = pk2(a[2 * pr][2], a[2 * pr][3]), p2 = pk2(a[2 * pr + 1][0], a[2 * pr + 1][1]), p3 = pk2(a[2 * pr + 1][2], a[2 * pr + 1][3]);
    o[pr][0] = hi ? p2 : p0; o[pr][1] = hi ? p3 : p1; o[pr][2] = hi ? p0 : p2; o[pr][3] = hi ? p1 : p3;
  }
  u32x4 sa, sb;
#pragma unroll
  for (int e = 0; e < 4; ++e) {
    sa[e] = __builtin_amdgcn_update_dpp(o[0][e], o[1][e], 0x128, 0xf, 0xc, false);
    sb[e] = __builtin_amdgcn_update_dpp(o[1][e], o[0][e], 0x128, 0xf, 0x3, false);
  }
  stc16(rxo, xoff, 0, sa); stc16(rxo, xoff, 8 * D_ * 2, sb);
  if (!stats) return;
#pragma unroll
  for (int pr = 0; pr < 2; ++pr) {
    const bf16x8 f = __builtin_bit_cast(bf16x8, o[pr]);
    sm = mfma(__builtin_bit_cast(bf16x8, ones4), f, sm);
    sq = mfma(f, f, sq);
  }
  const float dq = (m & 2) ? ((m & 1) ? sq[3] : sq[2]) : ((m & 1) ? sq[1] : sq[0]);
  if (g == (m >> 2)) { const u32x2 r = {__float_as_uint(sm[0]), __float_as_uint(fmaxf(dq - sm[0] * sm[0] * (1.f / 64.f), 0.f))}; stc8(rws, stoff, 0, r); }
}
DI unsigned xs_adj(int m) { return X_FULL ? (unsigned)((m >> 3) * (64 - 8 * D_ * 2)) : 0u; }
template <int N, int NI> DI void drain_bar() { WAITV(N * NI); BAR(); if constexpr (N > 0) drain_bar<N - 1, NI>(); }

#define STAMP_WID wid
#define ALOAD8C(dst, rs, voff, soff) asm volatile("buffer_load_dwordx2 %0, %1, %2, %3 offen sc1" : "=v"(dst) : "v"(voff), "s"(rs), "s"(soff) : "memory")

constexpr int K1_STAGE = 40960, K1_RED = 3 * K1_STAGE, K1_D = QDEF;
DI void p1_load(const Params& P, int l, LAS char* lds, int j) {
  const int lane = fresh_lane();
  const int bid = fresh_s(blockIdx.x), xcd = bid & 7, loc = bid >> 3;
  const int rt = (xcd & 1) * 8 + (loc & 7), hp = (xcd >> 1) * 4 + (loc >> 3);
  const int rot = l ? 0 : hp + rt;
  const rsrc_t rX = mk_rsrc(l ? P.out : P.x0, S_ * D_ * 2), rW = mk_rsrc(P.wqkv, D_ * 3072 * 2);
  const int r = lane >> 3, sl8 = lane & 7, f = ((r >> 1) & 1) | ((j & 1) << 1);
  const unsigned voffX = r * 2048 + ((sl8 ^ (r & 6)) * 16), voffW = r * 6144 + ((sl8 ^ (2 * f)) * 16);
  auto stX = [&](int t, int st) {
    const unsigned sx = (unsigned)((rt * 128 + j * 32) * 2048 + ((t + rot) & 15) * 128);
#pragma unroll
    for (int u = 0; u < 4; ++u) dma16c(rX, lds + st * K1_STAGE + (4 * j + u) * 1024, voffX, sx + u * 16384);
  };
  auto stW = [&](int t, int st, int p0 = 0, int p1 = 3) {
    const unsigned sw = (unsigned)((((t + rot) & 15) * 64 + j * 8) * 6144 + hp * 128);
#pragma unroll
    for (int s = 0; s < 2; ++s)
#pragma unroll
      for (int pnl = p0; pnl < p1; ++pnl) dma16(rW, lds + st * K1_STAGE + 16384 + pnl * 8192 + (4 * s + j) * 1024, voffW, sw + pnl * 2048 + s * 32 * 6144);
  };
  if (l) { stW(0, 0); stW(1, 1); l_wait(P.ws, 64 + C_X + rt, 16, lds, 5, j); stX(0, 0); stX(1, 1); WAITV(4); }
  else { stX(0, 0); stW(0, 0); stX(1, 1); stW(1, 1); WAITV(10); }
  BAR();
  stX(2, 2); stW(2, 2);
  WAITV(10); BAR();
  stX(3, 0); stW(3, 0);
  WAITV(10); BAR();
  stX(4, 1); stW(4, 1);
  WAITV(10); BAR();
  int st = 2;
  for (int t = 3; t < 14 - K1_D; ++t) {
    stX(t + 2, st); stW(t + 2, st);
    if (l == 0) pf_lines<4>((const char*)P.wo + ((bid * 4 + j) * 16 + ((t - 3) & 3) * 4) * 128);
    WAITV(10); BAR();
    st = st == 2 ? 0 : st + 1;
  }
  for (int t = 14 - K1_D; t < 14; ++t) {
    stX(t + 2, st); stW(t + 2, st, 1, 3);
    WAITV(8); BAR();
    st = st == 2 ? 0 : st + 1;
  }
  for (int t = 14; t < 14 + K1_D; ++t) {
    stX(t + 2 - K1_D, st); stW(t + 2 - K1_D, st, 0, 1);
    if (t == 16) BAR();
    WAITV(6); BAR();
    st = st == 2 ? 0 : st + 1;
  }
  WAITV(0); BAR();
  BAR(); BAR();
}
DI void p1_comp(const Params& P, int l, LAS char* lds, int wid) {
  STAMP_DECL STAMP_AT(0);
  const int bid = fresh_s(blockIdx.x), xcd = bid & 7, loc = bid >> 3;
  const int rt = (xcd & 1) * 8 + (loc & 7), hp = (xcd >> 1) * 4 + (loc >> 3);
  if (wid >= 4) {
    {
      const int lane = fresh_lane(), i4 = wid - 4, tid = i4 * 64 + lane;
      const brs_t rws = mk_brs(P.ws, WS_BYTES);
      int nb = 0;
      __builtin_amdgcn_s_setprio(HELP_PRIO);
      if (l == 0) {
        const u64 tag = call_tag(P.ws);
        if (wid == 4 && lane < 7) { const int w = lane < 6 ? bid * 6 + lane : (C_KV + hp) * NREP + (rt & (NREP - 1));
          if (lane == 6 || w < C_KV * NREP || w >= C_O * NREP) __hip_atomic_store(cnt_ptr(P.ws, w), 0u, __ATOMIC_RELAXED, __HIP_MEMORY_SCOPE_AGENT); }
        const int m = lane & 15, g = lane >> 4, q = (lane >> 2) & 3, p = lane & 3, kt = (hp + rt) & 15;
        const int xa0 = (i4 * 32 + m) * 128 + ((g ^ (m & 6)) * 16), xa1 = xa0 ^ 64;
        const int wa = 16384 + (8 * g + q) * 128 + (((2 * i4 + (p >> 1)) ^ (2 * (((q >> 1) & 1) | ((g & 1) << 1)))) * 16) + (p & 1) * 8;
        const bf16x8 ones = __builtin_bit_cast(bf16x8, ones_bf16());
        const f32x4 z4 = {0.f, 0.f, 0.f, 0.f};
        BAR();
#pragma unroll
        for (int mb = 0; mb < 2; ++mb) {
          const bf16x8 x0 = ld128(lds + xa0 + mb * 2048), x1 = ld128(lds + xa1 + mb * 2048);
          const f32x4 sm = mfma(ones, x1, mfma(ones, x0, z4)), sq = mfma(x1, x1, mfma(x0, x0, z4));
          const float dq = (m & 2) ? ((m & 1) ? sq[3] : sq[2]) : ((m & 1) ? sq[1] : sq[0]);
          if (g == (m >> 2)) { const u32x2 r = {__float_as_uint(sm[0]), __float_as_uint(fmaxf(dq - sm[0] * sm[0] * (1.f / 64.f), 0.f))}; stc8(rws, m * 8, WS_ST + (kt * 2048 + rt * 128 + i4 * 32 + mb * 16) * 8, r); }
        }
#pragma unroll
        for (int pnl = 0; pnl < 3; ++pnl) {
          const f32x4 c = mfma(ones, ldtr(lds + wa + pnl * 8192 + 4096, 512), mfma(ones, ldtr(lds + wa + pnl * 8192, 512), z4));
          if (lane < 16) __builtin_amdgcn_raw_buffer_store_b32(__float_as_uint(c[0]), rws, lane * 4, WS_CS + (kt * 3072 + pnl * 1024 + hp * 64 + i4 * 16) * 4, 16);
        }
        BAR(); BAR();
        WAITV(0);
        { unsigned old = 0;
          if (lane == 0) old = __hip_atomic_fetch_add((LAS unsigned*)(lds + L_SYNC + 64), 1u, __ATOMIC_RELAXED, __HIP_MEMORY_SCOPE_WORKGROUP);
          if (__builtin_amdgcn_readfirstlane(old) == 3 && lane < 2)
            __hip_atomic_store(lane ? tag_ptr(P.ws, T_CS + hp) + rt : tag_ptr(P.ws, T_X0 + rt) + hp, tag, __ATOMIC_RELAXED, __HIP_MEMORY_SCOPE_AGENT); }
#pragma unroll 1
        for (int i = 3; i < 9; ++i) BAR();
        if (wid < 6) tag_poll(tag_ptr(P.ws, wid == 4 ? T_X0 + rt : T_CS + hp), 16, tag);
        BAR();
        nb = 10;
      } else { BAR(); nb = 1; }
      u32x2 sv[8]; float cv[16];
#pragma unroll
      for (int i = 0; i < 8; ++i) sv[i] = __builtin_amdgcn_raw_buffer_load_b64(rws, (tid & 1) * 131072 + (tid >> 1) * 8, WS_ST + (unsigned)(2 * l) * 262144u + rt * 1024 + i * 16384, 16);
      if (l == 0) { const int ci = tid < 192 ? tid : 0;
#pragma unroll
        for (int i = 0; i < 16; ++i) cv[i] = __uint_as_float(__builtin_amdgcn_raw_buffer_load_b32(rws, ((ci >> 6) * 1024 + (ci & 63)) * 4, WS_CS + (i * 3072 + hp * 64) * 4, 16)); }
      BAR();
      const f32x2 mr = combine_stats<8, true>(sv);
      if (!(tid & 1)) *(LAS f32x2*)(lds + L_STATS + (tid >> 1) * 8) = mr;
      if (l == 0) { float c = 0.f;
#pragma unroll
        for (int i = 0; i < 16; ++i) c += cv[i];
        if (tid < 192) *(LAS float*)(lds + L_CS1 + tid * 4) = c; }
      WAITL0();
      __builtin_amdgcn_s_setprio(0);
#pragma unroll 1
      for (int i = nb + 1; i < 16 + K1_D; ++i) BAR();
    }
    unsigned look = 0;
    if (wid == 4) asm volatile("global_load_dword %0, %1, %2 sc1" : "=v"(look) : "v"(0), "s"(rep_ptr(P.ws, 64 * l + C_KV + hp)) : "memory");
    BAR();
    STAMP_AT(0);
    volatile LAS unsigned* relay = (volatile LAS unsigned*)(lds + L_SYNC + 56);
    if (wid == 4) {
      asm volatile("s_waitcnt vmcnt(0)" : "+v"(look) :: "memory");
      if ((unsigned)__builtin_amdgcn_readfirstlane(look) < 16u) poll_ge(rep_ptr(P.ws, 64 * l + C_KV + hp), 16);
      *relay = l + 1;
    }
    else { unsigned spins = 0; while ((unsigned)__builtin_amdgcn_readfirstlane(*relay) < (unsigned)(l + 1) && ++spins < (1u << 24)) __builtin_amdgcn_s_sleep(1); }
    FENCE();
    STAMP_AT(1);
    const int lane = fresh_lane(), hw = (wid >> 1) & 1, wr = wid & 1;
    const brs_t rws = mk_brs(P.ws, WS_BYTES);
    const unsigned src = WS_KVP + (2 * hp + hw) * 2176;
    u32x4 v[16];
#pragma unroll
    for (int i = 0; i < 16; ++i) v[i] = ldc16(rws, lane * 16, src + i * 32 * 2176 + wr * 1024);
    const u32x2 zv = __builtin_amdgcn_raw_buffer_load_b64(rws, (lane >> 2) * (32 * 2176) + (lane & 3) * 8, src + 2048 + wr * 32, 16);
    float s[8] = {0.f, 0.f, 0.f, 0.f, 0.f, 0.f, 0.f, 0.f};
#pragma unroll
    for (int i = 0; i < 16; ++i)
#pragma unroll
      for (int e = 0; e < 4; ++e) { s[2 * e] += bflo(v[i][e]); s[2 * e + 1] += bfhi(v[i][e]); }
#pragma unroll
    for (int b = 0; b < 2; ++b) {
      const int e = 16 * b + (lane & 15), i16 = (e >> 3) * 4 + (e & 3), bp = (e >> 2) & 1;
      const u32x2 o = {pk2(s[4 * b], s[4 * b + 1]), pk2(s[4 * b + 2], s[4 * b + 3])};
      *(LAS u32x2*)(lds + L_KV + hw * 2112 + bp * 1024 + ((lane >> 4) * 16 + i16) * 16 + 8 * wr) = o;
    }
    float z[4] = {bflo(zv[0]), bfhi(zv[0]), bflo(zv[1]), bfhi(zv[1])};
#pragma unroll
    for (int e = 0; e < 4; ++e) {
      z[e] += __uint_as_float(__builtin_amdgcn_update_dpp(0u, __float_as_uint(z[e]), 0x124, 0xf, 0xf, false));
      z[e] += __uint_as_float(__builtin_amdgcn_update_dpp(0u, __float_as_uint(z[e]), 0x128, 0xf, 0xf, false));
      z[e] = xsum(z[e]);
    }
    if (lane < 4) { const u32x2 oz = {pk2(z[0], z[1]), pk2(z[2], z[3])}; *(LAS u32x2*)(lds + L_KV + hw * 2112 + 2048 + lane * 16 + 8 * wr) = oz; }
    WAITL0();
    BAR(); BAR();
    return;
  }
  const int wr = wid >> 1, wc = wid & 1;
  f32x4 acc[3][4][2];
  {
    const int lane = fresh_lane(), m = lane & 15, g = lane >> 4, q = (lane >> 2) & 3, p = lane & 3;
    int xa0 = (wr * 64 + m) * 128 + ((g ^ (m & 6)) * 16), xa1 = xa0 ^ 64;
    int wa0 = 16384 + (8 * g + q) * 128 + (((4 * wc + (p >> 1)) ^ (2 * (((q >> 1) & 1) | ((g & 1) << 1)))) * 16) + (p & 1) * 8, wa1 = wa0 ^ 32;
#pragma unroll
    for (int a = 0; a < 3; ++a)
#pragma unroll
      for (int b = 0; b < 4; ++b)
#pragma unroll
        for (int c = 0; c < 2; ++c) acc[a][b][c] = (f32x4){0.f, 0.f, 0.f, 0.f};
    bf16x8 xf[4], wf[2][3][2];
    auto step = [&](int cur, bool q_now, bool q_next) {
      LAS char* xp = lds + (cur ? xa0 : xa1); LAS char* w0 = lds + wa0 + (cur ? 0 : 4096); LAS char* w1 = lds + wa1 + (cur ? 0 : 4096);
#pragma unroll
      for (int mb = 0; mb < 4; ++mb) {
        const bf16x8 x = xf[mb];
        if (q_next) {
          if (mb == 1) { wf[cur ^ 1][0][0] = ldtr(w0, 512); xf[0] = ld128(xp); wf[cur ^ 1][1][0] = ldtr(w0 + 8192, 512); }
          if (mb == 2) { wf[cur ^ 1][2][0] = ldtr(w0 + 16384, 512); xf[1] = ld128(xp + 2048); wf[cur ^ 1][0][1] = ldtr(w1, 512); }
          if (mb == 3) { wf[cur ^ 1][1][1] = ldtr(w1 + 8192, 512); xf[2] = ld128(xp + 4096); wf[cur ^ 1][2][1] = ldtr(w1 + 16384, 512); }
        } else {
          if (mb == 1) { wf[cur ^ 1][1][0] = ldtr(w0 + 8192, 512); wf[cur ^ 1][2][0] = ldtr(w0 + 16384, 512); }
          if (mb == 2) { xf[0] = ld128(xp); wf[cur ^ 1][1][1] = ldtr(w1 + 8192, 512); xf[1] = ld128(xp + 2048); }
          if (mb == 3) { wf[cur ^ 1][2][1] = ldtr(w1 + 16384, 512); xf[2] = ld128(xp + 4096); }
        }
#pragma unroll
        for (int nb = 0; nb < 2; ++nb) {
          if (q_now) acc[0][mb][nb] = mfma(wf[cur][0][nb], x, acc[0][mb][nb]);
          acc[1][mb][nb] = mfma(x, wf[cur][1][nb], acc[1][mb][nb]);
          acc[2][mb][nb] = mfma(x, wf[cur][2][nb], acc[2][mb][nb]);
        }
        if (mb == 0 && cur) { SB0(); WAITL0(); BAR(); }
        if (mb == 3) xf[3] = ld128(xp + 6144);
        if (mb > 0) { _Pragma("unroll") for (int i_ = 0; i_ < 6; ++i_) { SGB(0x8, 1); SGB(0x100, 1); } }
        SB0();
      }
    };
    auto advance = [&](int& st) {
      const int d = st == 2 ? -2 * K1_STAGE : K1_STAGE; st = st == 2 ? 0 : st + 1;
      xa0 += d; xa1 += d; wa0 += d; wa1 += d; PIN(xa0); PIN(xa1); PIN(wa0); PIN(wa1);
    };
    BAR();
    STAMP_AT(1);
#pragma unroll
    for (int mb = 0; mb < 4; ++mb) xf[mb] = ld128(lds + xa0 + mb * 2048);
#pragma unroll
    for (int pnl = 0; pnl < 3; ++pnl) { wf[0][pnl][0] = ldtr(lds + wa0 + pnl * 8192, 512); wf[0][pnl][1] = ldtr(lds + wa1 + pnl * 8192, 512); }
    SB0();
    int st = 0;
    for (int t = 0; t < 16 - K1_D; ++t) { step(0, true, true); advance(st); step(1, true, true); }
    for (int t = 16 - K1_D; t < 16; ++t) { step(0, false, false); advance(st); step(1, false, false); }
  }
  STAMP_AT(2);
  const int lane = fresh_lane(), m = lane & 15, g = lane >> 4;
  const brs_t rws = mk_brs(P.ws, WS_BYTES);
  const int head = 2 * hp + wc, trow = wr * 64;
  const LAS f32x2* stt = (const LAS f32x2*)(lds + L_STATS);
  const LAS float* csl = (const LAS float*)(lds + L_CS1);
  const u32x4 ones4 = ones_bf16();
  f32x4 kvr[6];
#pragma unroll
  for (int i = 0; i < 6; ++i) kvr[i] = (f32x4){0.f, 0.f, 0.f, 0.f};
  float ck[2], cv[2];
#pragma unroll
  for (int nb = 0; nb < 2; ++nb) { ck[nb] = csl[64 + wc * 32 + nb * 16 + m]; cv[nb] = csl[128 + wc * 32 + nb * 16 + m]; }
#pragma unroll
  for (int mp = 0; mp < 2; ++mp) {
    u32x4 pkf[2], vf[2];
#pragma unroll
    for (int hb = 0; hb < 2; ++hb) {
      const int mb = 2 * mp + hb;
      const f32x4 a0 = *(const LAS f32x4*)(stt + trow + mb * 16 + 4 * g), a1 = *(const LAS f32x4*)(stt + trow + mb * 16 + 4 * g + 2);
      const float mean[4] = {a0[0], a0[2], a1[0], a1[2]}, rstd[4] = {a0[1], a0[3], a1[1], a1[3]};
#pragma unroll
      for (int nb = 0; nb < 2; ++nb) {
        float kk[4], vv[4];
#pragma unroll
        for (int e = 0; e < 4; ++e) { kk[e] = phi_ln(acc[1][mb][nb][e] - mean[e] * ck[nb], rstd[e], rstd[e] * 1.44269504f); vv[e] = rstd[e] * (acc[2][mb][nb][e] - mean[e] * cv[nb]); }
        pkf[nb][2 * hb] = pk2(kk[0], kk[1]); pkf[nb][2 * hb + 1] = pk2(kk[2], kk[3]);
        vf[nb][2 * hb] = pk2(vv[0], vv[1]); vf[nb][2 * hb + 1] = pk2(vv[2], vv[3]);
      }
      SB0();
    }
#pragma unroll
    for (int a = 0; a < 2; ++a) {
#pragma unroll
      for (int b = 0; b < 2; ++b) kvr[a * 3 + b] = mfma(__builtin_bit_cast(bf16x8, pkf[a]), __builtin_bit_cast(bf16x8, vf[b]), kvr[a * 3 + b]);
      kvr[a * 3 + 2] = mfma(__builtin_bit_cast(bf16x8, pkf[a]), __builtin_bit_cast(bf16x8, ones4), kvr[a * 3 + 2]);
    }
  }
  f32x4 own[3];
#pragma unroll
  for (int i = 0; i < 3; ++i) {
    f32x4 snd;
#pragma unroll
    for (int e = 0; e < 4; ++e) { snd[e] = wr ? kvr[i][e] : kvr[3 + i][e]; own[i][e] = wr ? kvr[3 + i][e] : kvr[i][e]; }
    *(LAS f32x4*)(lds + K1_RED + wid * 3072 + i * 1024 + lane * 16) = snd;
  }
  WAITL0();
  BAR();
#pragma unroll
  for (int i = 0; i < 3; ++i) own[i] += *(LAS f32x4*)(lds + K1_RED + (wid ^ 2) * 3072 + i * 1024 + lane * 16);
  { const unsigned dst = WS_KVP + (rt * 32 + head) * 2176;
    const u32x4 o = {pk2(own[0][0], own[0][1]), pk2(own[0][2], own[0][3]), pk2(own[1][0], own[1][1]), pk2(own[1][2], own[1][3])};
    stc16(rws, lane * 16, dst + wr * 1024, o);
    const u32x2 oz = {pk2(own[2][0], own[2][1]), pk2(own[2][2], own[2][3])};
    if (m == 0) stc8(rws, g * 8, dst + 2048 + wr * 32, oz); }
  { const int q = (lane >> 2) & 3, p = lane & 3;
    const int xa0 = (wr * 64 + m) * 128 + ((g ^ (m & 6)) * 16), xa1 = xa0 ^ 64;
    const int wa0 = 16384 + (8 * g + q) * 128 + (((4 * wc + (p >> 1)) ^ (2 * (((q >> 1) & 1) | ((g & 1) << 1)))) * 16) + (p & 1) * 8, wa1 = wa0 ^ 32;
    bf16x8 xg[2][2][4], wg[2][2][2];
    auto fetchq = [&](int set, int st) {
      LAS char* b = lds + st * K1_STAGE;
#pragma unroll
      for (int s = 0; s < 2; ++s) {
        wg[set][s][0] = ldtr(b + wa0 + s * 4096, 512); wg[set][s][1] = ldtr(b + wa1 + s * 4096, 512);
#pragma unroll
        for (int mb = 0; mb < 4; ++mb) xg[set][s][mb] = ld128(b + (s ? xa1 : xa0) + mb * 2048);
      }
    };
    auto mmaq = [&](int set) {
#pragma unroll
      for (int s = 0; s < 2; ++s)
#pragma unroll
        for (int mb = 0; mb < 4; ++mb)
#pragma unroll
          for (int nb = 0; nb < 2; ++nb) acc[0][mb][nb] = mfma(wg[set][s][nb], xg[set][s][mb], acc[0][mb][nb]);
    };
#define K1Q_MIX() do { _Pragma("unroll") for (int i_ = 0; i_ < 8; ++i_) { SGB(0x8, 1); SGB(0x100, 2); } SGB(0x8, 8); SB0(); } while (0)
    fetchq(0, 16 % 3);
    SB0();
    c_signal<true>(P.ws, 64 * l + C_KV + hp, lds, 5 * l + 0, 4);
#pragma unroll
    for (int u = 1; u < K1_D; ++u) { WAITL0(); BAR(); fetchq(u & 1, (16 + u) % 3); mmaq((u & 1) ^ 1); K1Q_MIX(); }
    mmaq((K1_D - 1) & 1); }
  f32x4 c4[2];
#pragma unroll
  for (int nb = 0; nb < 2; ++nb) c4[nb] = *(const LAS f32x4*)(csl + wc * 32 + nb * 16 + 4 * g);
  u32x4 pq[4];
#pragma unroll
  for (int mb = 0; mb < 4; ++mb) {
    const f32x2 mr = stt[trow + mb * 16 + m];
    const float rs2 = mr[1] * 1.44269504f;
#pragma unroll
    for (int nb = 0; nb < 2; ++nb) {
      f32x4 v;
#pragma unroll
      for (int e = 0; e < 4; ++e) v[e] = phi_ln(acc[0][mb][nb][e] - mr[0] * c4[nb][e], mr[1], rs2);
      pq[mb][2 * nb] = pk2(v[0], v[1]); pq[mb][2 * nb + 1] = pk2(v[2], v[3]);
    }
    PIN(pq[mb]);
  }
  STAMP_END(1 + 5 * l);
  STAMP_AT(0);
  BAR();
  STAMP_AT(1); STAMP_AT(2);
  { LAS char* kb = lds + L_KV + wc * 2112;
    const bf16x8 k0 = ld128(kb + lane * 16), k1 = ld128(kb + 1024 + lane * 16), zf = ld128(kb + 2048 + g * 16);
    const f32x4 z4 = {0.f, 0.f, 0.f, 0.f};
#pragma unroll
    for (int mb = 0; mb < 4; ++mb) {
      const bf16x8 x = __builtin_bit_cast(bf16x8, pq[mb]);
      const f32x4 dn = mfma(zf, x, z4), d0 = mfma(k0, x, z4), d1 = mfma(k1, x, z4);
      const float rr = __builtin_amdgcn_rcpf(dn[0] + 1e-6f);
      const u32x4 o = {pk2(d0[0] * rr, d0[1] * rr), pk2(d0[2] * rr, d0[3] * rr), pk2(d1[0] * rr, d1[1] * rr), pk2(d1[2] * rr, d1[3] * rr)};
      stc16(rws, lane * 16, WS_PQ + ((rt * 8 + wr * 4 + mb) * 32 + head) * 1024, o);
    } }
  BAR();
  c_signal<true>(P.ws, 64 * l + C_O + (rt >> 1), lds, 5 * l + 1, 4);
  STAMP_END(2 + 5 * l);
}

constexpr int K2_NT = 16, K2_STAGE = 24576, K2_NS = 6;
template <bool P1B> DI void p2_load(const Params& P, int l, LAS char* lds, int j) {
  const int lane = fresh_lane();
  const int bid = fresh_s(blockIdx.x), xcd = bid & 7, loc = bid >> 3;
  const int rt = (xcd & 3) * 4 + (loc & 3), ct = (xcd >> 2) * 8 + (loc >> 2);
  const int rot = ROT_K2 ? ((loc >> 2) + 2 * (loc & 3)) & 7 : 0;
  const rsrc_t rA = mk_rsrc(P.ws, WS_BYTES), rW = mk_rsrc(P.wo, D_ * D_ * 2);
  const int r = lane >> 3, sl8 = lane & 7, f = ((r >> 1) & 1) | ((j & 1) << 1);
  const unsigned voffW = r * 2048 + ((sl8 ^ (2 * f)) * 16), voffL = lane * 16;
  auto stX = [&](int t, int sl) {
    const unsigned sa = WS_PQ + (unsigned)((rt * 8 + 2 * j) * 32768 + ((t & ~7) | ((t + rot) & 7)) * 2048);
#pragma unroll
    for (int u = 0; u < 4; ++u) dma16c(rA, lds + sl * K2_STAGE + (4 * j + u) * 1024, voffL, sa + (u >> 1) * 32768 + (u & 1) * 1024);
  };
  auto stW = [&](int t, int sl) {
    const unsigned sw = (unsigned)((((t & ~7) | ((t + rot) & 7)) * 64 + j * 8) * 2048 + ct * 128);
#pragma unroll
    for (int u = 0; u < 2; ++u) dma16(rW, lds + sl * K2_STAGE + 16384 + (j + 4 * u) * 1024, voffW, sw + u * 32 * 2048);
  };
#pragma unroll
  for (int t = 0; t < 5; ++t) stW(t, t);
  l_wait(P.ws, 64 * l + C_O + (rt >> 1), 32, lds, 5 * l + 2, j);
#pragma unroll
  for (int t = 0; t < 5; ++t) stX(t, t);
  WAITV(16); BAR(); stX(5, 5); stW(5, 5);
  WAITV(18); BAR(); stX(6, 0); stW(6, 0);
  WAITV(20); BAR(); stX(7, 1); stW(7, 1);
  WAITV(22); BAR(); stX(8, 2); stW(8, 2);
  int sl = 3;
  for (int t = 4; t < K2_NT - 5; ++t) {
    WAITV(24); BAR();
    stX(t + 5, sl); stW(t + 5, sl);
    sl = sl == K2_NS - 1 ? 0 : sl + 1;
  }
  drain_bar<4, 6>();
  BAR();
}
DI void p2_comp(const Params& P, int l, LAS char* lds, int wid) {
  STAMP_DECL STAMP_AT(0);
  if (wid >= 4) {
#pragma unroll 1
    for (int i = 0; i < K2_NT + 1; ++i) BAR();
    return;
  }
  const int lane = fresh_lane();
  const int bid = fresh_s(blockIdx.x), xcd = bid & 7, loc = bid >> 3;
  const int rt = (xcd & 3) * 4 + (loc & 3), ct = (xcd >> 2) * 8 + (loc >> 2);
  const int m = lane & 15, g = lane >> 4, q = (lane >> 2) & 3, p = lane & 3;
  const int row0 = rt * 128 + wid * 32 + m;
  const unsigned off0 = (unsigned)(row0 * D_ + ct * 64 + 8 * g) * 2;
  int wa[4];
  { const int f = ((q >> 1) & 1) | ((g & 1) << 1);
#pragma unroll
    for (int nb = 0; nb < 4; ++nb) wa[nb] = 16384 + (8 * g + q) * 128 + (((4 * (nb >> 1) + p) ^ (2 * f)) * 16) + (((nb & 1) ^ (p >> 1)) * 8); }
  const int xa = wid * 4096 + lane * 16;
  f32x4 acc[2][4];
  { const brs_t rxi = mk_brs(l ? P.out : P.x0, S_ * D_ * 2);
    u32x4 res[2][2];
#pragma unroll
    for (int mb = 0; mb < 2; ++mb) { res[mb][0] = ldc16(rxi, off0, mb * 32768); res[mb][1] = ldc16(rxi, off0, mb * 32768 + 64); }
#pragma unroll
    for (int mb = 0; mb < 2; ++mb) res_unpack(acc[mb], res[mb], g >= 2, false); }
  bf16x8 xf[2][2][2], wf[2][2][4];
  auto fetch = [&](int set, int sl) {
    LAS char* b = lds + sl * K2_STAGE;
#pragma unroll
    for (int s = 0; s < 2; ++s) {
      xf[set][s][0] = ld128(b + xa + s * 1024); xf[set][s][1] = ld128(b + xa + s * 1024 + 2048);
#pragma unroll
      for (int nb = 0; nb < 4; ++nb) wf[set][s][nb] = ldtr(b + wa[nb] + s * 4096, 512);
    }
  };
  auto mma = [&](int set) {
#pragma unroll
    for (int s = 0; s < 2; ++s)
#pragma unroll
      for (int nb = 0; nb < 4; ++nb)
#pragma unroll
        for (int mb = 0; mb < 2; ++mb) acc[mb][nb] = mfma(wf[set][s][nb], xf[set][s][mb], acc[mb][nb]);
  };
#define K2_MIX() do { _Pragma("unroll") for (int i_ = 0; i_ < 10; ++i_) { SGB(0x8, 1); SGB(0x100, 2); } SGB(0x8, 6); SB0(); } while (0)
  BAR();
  STAMP_AT(1);
  fetch(0, 0);
  SB0();
  int sl = 1;
  for (int t = 1; t < K2_NT - 1; t += 2) {
    WAITL0(); BAR();
    fetch(1, sl); mma(0); K2_MIX();
    sl = sl == K2_NS - 1 ? 0 : sl + 1;
    WAITL0(); BAR();
    fetch(0, sl); mma(1); K2_MIX();
    sl = sl == K2_NS - 1 ? 0 : sl + 1;
  }
  WAITL0(); BAR();
  fetch(1, sl); mma(0); K2_MIX();
  mma(1);
  STAMP_AT(2);
  const brs_t rws = mk_brs(P.ws, WS_BYTES);
#pragma unroll
  for (int mb = 0; mb < 2; ++mb)
    x_store(acc[mb], rws, WS_X1 + off0 + xs_adj(m) + mb * 32768, rws, WS_ST + (unsigned)(2 * l + 1) * 262144u + (ct * 2048 + row0 + mb * 16) * 8, m, g, true);
  BAR();
  c_signal<true>(P.ws, 64 * l + C_X1 + (rt >> 1), lds, 5 * l + 2, 4);
  STAMP_END(3 + 5 * l);
}

DI void p3_load(const Params& P, int l, LAS char* lds, int j) {
  const int lane = fresh_lane(), tid = j * 64 + lane;
  const int bid = fresh_s(blockIdx.x), xcd = bid & 7, loc = bid >> 3;
  const int rt = (xcd & 1) * 4 + (loc & 3), ct = (xcd >> 1) * 8 + (loc >> 2);
  const int rot = l ? 0 : 2 * rt;
  const int mt = j & 1, hi2 = j >> 1;
  unsigned voffA, voffB;
  { const int r = lane >> 3, sl8 = lane & 7; voffA = r * 2048 + ((sl8 ^ (r & 6)) * 16);
    const int qq = lane >> 4, sl = lane & 15, f = qq | (hi2 << 2), ch = sl ^ (2 * f) ^ mt; voffB = ch < 14 ? (unsigned)(qq * 7168 + ch * 16) : OOB_V; }
  const rsrc_t rS = mk_rsrc(P.ws, WS_BYTES), rW = mk_rsrc(mt ? P.wu : P.wg, D_ * F_ * 2);
  auto stA = [&](int half, int t) {
    const unsigned sb = WS_X1 + (unsigned)((rt * 256 + half * 128 + j * 32) * 2048 + ((t + rot) & 15) * 128);
#pragma unroll
    for (int u = 0; u < 4; ++u) dma16c(rS, lds + half * 32768 + (t & 1) * 16384 + (4 * j + u) * 1024, voffA, sb + u * 16384);
  };
  auto stB = [&](int s, int t) {
    const unsigned sb = (unsigned)((((t + rot) & 15) * 64 + s * 32 + hi2 * 8) * 7168 + ct * 224);
#pragma unroll
    for (int u = 0; u < 4; ++u) { const int i = (u & 1) + 4 * (u >> 1);
      dma16(rW, lds + 65536 + ((t & 1) * 2 + s) * 16384 + mt * 8192 + (i + 2 * hi2) * 1024, voffB, sb + i * 4 * 7168); }
  };
  stB(0, 0); stB(1, 0); stB(0, 1);
  l_wait(P.ws, 64 * l + C_X1 + rt, 32, lds, 5 * l + 3, j);
  stA(0, 0); stA(1, 0); stA(0, 1);
  u32x2 sv[16];
  { const unsigned so = WS_ST + (unsigned)(2 * l + 1) * 262144u + rt * 2048, vo = tid * 8;
#pragma unroll
    for (int i = 0; i < 16; ++i) ALOAD8C(sv[i], rS, vo, so + i * 16384); }
  int ca[4]; f32x4 cs[4]; bf16x8 cf[4];
  { const int g = lane >> 4, q = (lane >> 2) & 3, p = lane & 3, f2 = 2 * (q | ((g & 1) << 2));
#pragma unroll
    for (int u = 0; u < 4; ++u) { const int blk = min(j + 4 * u, 13), mm = blk >= 7; ca[u] = 65536 + mm * 8192 + (8 * g + q) * 256 + (((2 * (blk - 7 * mm) + (p >> 1)) ^ f2 ^ mm) * 16) + (p & 1) * 8;
      cs[u] = (f32x4){0.f, 0.f, 0.f, 0.f}; } }
  const bf16x8 ones = __builtin_bit_cast(bf16x8, ones_bf16());
  auto csr = [&](int piece) { if (l == 0) {
#pragma unroll
      for (int u = 0; u < 4; ++u) cf[u] = ldtr(lds + ca[u] + piece * 16384, 1024); } };
  auto csd = [&]() { if (l == 0) {
#pragma unroll
      for (int u = 0; u < 4; ++u) { cs[u] = mfma(ones, cf[u], cs[u]); PIN(cs[u]); } } };
  WAITV(24); BAR();
  stA(1, 1); csr(0); WAITV(24); BAR();
  stB(1, 1); csd(); BAR();
  stB(0, 2); csr(1); BAR();
  stA(0, 2); csd(); WAITV(16); BAR();
  stA(1, 2); csr(2);
#pragma unroll
  for (int i = 0; i < 16; ++i) PIN(sv[i]);
  *(LAS f32x2*)(lds + L_STATS + tid * 8) = combine_stats<16, false>(sv);
  WAITL0();
  WAITV(16); BAR();
  stB(1, 2); csd(); WAITV(16); BAR();
  stB(0, 3); csr(3); WAITV(16); BAR();
  csd();
  if (l == 0) { const brs_t rws = mk_brs(P.ws, WS_BYTES);
#pragma unroll
    for (int u = 0; u < 4; ++u) if (lane < 16 && j + 4 * u < 14) __builtin_amdgcn_raw_buffer_store_b32(__float_as_uint(cs[u][0]), rws, ((j + 4 * u) * 16 + lane) * 4, WS_CS + (16 * 3072 + rt * 7168 + ct * 224) * 4, 16); }
  FENCE();
  stA(0, 3); WAITV(16); BAR();
  stA(1, 3); WAITV(16); BAR();
  stB(1, 3); WAITV(16); BAR();
  stB(0, 4); WAITV(16); BAR();
  stA(0, 4); WAITV(16);
  if (l == 0) {
    unsigned old = 0;
    if (lane == 0) old = __hip_atomic_fetch_add((LAS unsigned*)(lds + L_SYNC + 68), 1u, __ATOMIC_RELAXED, __HIP_MEMORY_SCOPE_WORKGROUP);
    if (__builtin_amdgcn_readfirstlane(old) == 3 && lane == 0) __hip_atomic_fetch_add(cnt_ptr(P.ws, ct), 1u, __ATOMIC_RELAXED, __HIP_MEMORY_SCOPE_AGENT);
  }
  BAR();
  for (int t = 3; t < 14; ++t) {
    const char* pfp = (const char*)P.wd + ((bid * 4 + j) * 56 + (t - 3) * 7) * 128;
    const bool pf = l == 0 && t < 11;
    stA(1, t + 1); if (pf) pf_lines<2>(pfp); WAITV(16); BAR();
    stB(1, t + 1); if (pf) pf_lines<2>(pfp + 256); WAITV(16); BAR();
    stB(0, t + 2); if (pf) pf_lines<2>(pfp + 512); WAITV(16); BAR();
    stA(0, t + 2); if (pf) pf_lines<1>(pfp + 768); WAITV(16); BAR();
  }
  stA(1, 15); WAITV(16); BAR();
  stB(1, 15); WAITV(16); BAR();
  WAITV(12); BAR();
  WAITV(8); BAR();
  WAITV(4); BAR();
  WAITV(0);
  if (l == 0 && j == 0) poll_ge(cnt_ptr(P.ws, ct), 8);
  BAR();
  float cv[8];
  if (l == 0) { const brs_t rws = mk_brs(P.ws, WS_BYTES);
#pragma unroll
    for (int i = 0; i < 8; ++i) cv[i] = __uint_as_float(__builtin_amdgcn_raw_buffer_load_b32(rws, min(tid, 223) * 4, WS_CS + (16 * 3072 + i * 7168 + ct * 224) * 4, 16)); }
  BAR();
  if (l == 0) { const float c = ((cv[0] + cv[1]) + (cv[2] + cv[3])) + ((cv[4] + cv[5]) + (cv[6] + cv[7])); if (tid < 224) *(LAS float*)(lds + L_CS + tid * 4) = c; WAITL0(); }
  BAR();
  BAR();
}
DI void p3_comp(const Params& P, int l, LAS char* lds, int wid) {
  STAMP_DECL STAMP_AT(0);
  const int bid = fresh_s(blockIdx.x), xcd = bid & 7, loc = bid >> 3;
  const int rt = (xcd & 1) * 4 + (loc & 3), ct = (xcd >> 1) * 8 + (loc >> 2);
  const int grp = wid >> 2, wrl = (wid >> 1) & 1, wc = wid & 1;
  f32x4 acc[4][7];
  {
    const int lane = fresh_lane();
    const int m = lane & 15, g = lane >> 4, q = (lane >> 2) & 3, p = lane & 3;
    int xa[2], wa[7];
#pragma unroll
    for (int s = 0; s < 2; ++s) xa[s] = grp * 32768 + (wrl * 64 + m) * 128 + (((4 * s + g) ^ (m & 6)) * 16);
    { const int f2 = 2 * (q | ((g & 1) << 2)), ro = 65536 + (8 * g + q) * 256, c0 = 7 * wc;
#pragma unroll
      for (int mm = 0; mm < 2; ++mm) {
#pragma unroll
        for (int b = 0; b < 2; ++b) wa[3 * mm + b] = ro + mm * 8192 + (((c0 + p) ^ f2 ^ mm) * 16) + ((b ^ (p >> 1)) * 8);
        wa[3 * mm + 2] = ro + mm * 8192 + (((c0 + 4 + (p >> 1)) ^ f2 ^ mm) * 16) + (p & 1) * 8;
      }
      wa[6] = ro + (p >> 1) * 8192 + (((c0 + 6) ^ f2 ^ (p >> 1)) * 16) + (p & 1) * 8; }
#pragma unroll
    for (int a = 0; a < 4; ++a)
#pragma unroll
      for (int b = 0; b < 7; ++b) acc[a][b] = (f32x4){0.f, 0.f, 0.f, 0.f};
    BAR();
    STAMP_AT(1);
    if (grp == 1) BAR();
    for (int t2 = 0; t2 < 16; t2 += 2) {
#pragma unroll
      for (int buf = 0; buf < 2; ++buf) {
#pragma unroll
        for (int s = 0; s < 2; ++s) {
          bf16x8 xf[4], wf[7];
#pragma unroll
          for (int mb = 0; mb < 4; ++mb) xf[mb] = ld128(lds + xa[s] + buf * 16384 + mb * 2048);
#pragma unroll
          for (int T = 0; T < 7; ++T) wf[T] = ldtr(lds + wa[T] + (buf * 2 + s) * 16384, 1024);
          WAITL0();
          BAR();
          __builtin_amdgcn_s_setprio(1);
#pragma unroll
          for (int mb = 0; mb < 4; ++mb)
#pragma unroll
            for (int T0 = 0; T0 < 7; ++T0) { const int T = (SNAKE && (mb & 1)) ? 6 - T0 : T0; acc[mb][T] = mfma(wf[T], xf[mb], acc[mb][T]); }
          __builtin_amdgcn_s_setprio(0);
          if (buf == 0 || s == 0 || t2 < 14 || grp == 0) BAR();
        }
      }
    }
  }
  STAMP_AT(2);
  const int tz = fresh_lane();
  const int m = tz & 15, g = tz >> 4;
  const LAS f32x2* stt = (const LAS f32x2*)(lds + L_STATS);
  const LAS float* csl = (const LAS float*)(lds + L_CS);
  f32x4 c4[7];
#pragma unroll
  for (int mm = 0; mm < 2; ++mm) {
#pragma unroll
    for (int b = 0; b < 2; ++b) c4[3 * mm + b] = *(const LAS f32x4*)(csl + mm * 112 + wc * 56 + 8 * g + 4 * (b ^ (g >> 1)));
    c4[3 * mm + 2] = *(const LAS f32x4*)(csl + mm * 112 + wc * 56 + 32 + 4 * g);
  }
  c4[6] = *(const LAS f32x4*)(csl + (g >> 1) * 112 + wc * 56 + 48 + 4 * (g & 1));
  const bool hi = g >= 2;
  const brs_t rws = mk_brs(P.ws, WS_BYTES);
  const unsigned hb = WS_H + (unsigned)(rt * 16 + grp * 8 + wrl * 4) * 114688u + (unsigned)(7 * (2 * ct + wc)) * 256u;
  const unsigned v16 = m * 16 + g * 256, v8 = m * 16 + (g >> 1) * 256 + (g & 1) * 114688, vz = m * 16 + ((g >> 1) + 2 * (g & 1)) * 114688;
  f32x4 zv[4];
  u32x2 h2[4];
#pragma unroll
  for (int mb = 0; mb < 4; ++mb) {
    const f32x2 mr = stt[grp * 128 + wrl * 64 + mb * 16 + m];
    const float r2 = mr[1] * mr[1];
    u32x2 hv[3];
#pragma unroll
    for (int b = 0; b < 3; ++b) {
      const f32x4 hh = ((acc[mb][b] - mr[0] * c4[b]) * r2) * (acc[mb][3 + b] - mr[0] * c4[3 + b]);
      hv[b][0] = pk2(hh[0], hh[1]); hv[b][1] = pk2(hh[2], hh[3]);
    }
    zv[mb] = (acc[mb][6] - mr[0] * c4[6]) * mr[1];
    const u32x4 o = {hi ? hv[1][0] : hv[0][0], hi ? hv[1][1] : hv[0][1], hi ? hv[0][0] : hv[1][0], hi ? hv[0][1] : hv[1][1]};
    stc16(rws, v16, hb + mb * 114688, o);
    h2[mb] = hv[2];
    if (mb & 1) {
      const auto s0 = __builtin_amdgcn_permlane16_swap(h2[mb - 1][0], h2[mb][0], false, false), s1 = __builtin_amdgcn_permlane16_swap(h2[mb - 1][1], h2[mb][1], false, false);
      const u32x4 o2 = {s0[0], s1[0], s0[1], s1[1]};
      stc16(rws, v8, hb + (mb - 1) * 114688 + 1024, o2);
    }
  }
  { u32x2 oz[2];
#pragma unroll
    for (int mp = 0; mp < 2; ++mp) {
      float hh[4];
#pragma unroll
      for (int e = 0; e < 4; ++e) {
        const auto r = __builtin_amdgcn_permlane32_swap(__float_as_uint(zv[2 * mp][e]), __float_as_uint(zv[2 * mp + 1][e]), false, false);
        hh[e] = __uint_as_float(r[0]) * __uint_as_float(r[1]);
      }
      oz[mp][0] = pk2(hh[0], hh[1]); oz[mp][1] = pk2(hh[2], hh[3]);
    }
    const auto s0 = __builtin_amdgcn_permlane16_swap(oz[0][0], oz[1][0], false, false), s1 = __builtin_amdgcn_permlane16_swap(oz[0][1], oz[1][1], false, false);
    const u32x4 o2 = {s0[0], s1[0], s0[1], s1[1]};
    stc16(rws, vz, hb + 1536, o2); }
  BAR();
  c_signal<true>(P.ws, 64 * l + C_H + rt * 2 + (ct >> 4), lds, 5 * l + 3, 8);
  STAMP_END(4 + 5 * l);
}

constexpr int K4_NT = 28, K4_STAGE = 32768, K4_NS = 4;
DI void p4_load(const Params& P, int l, LAS char* lds, int j) {
  const int lane = fresh_lane();
  const int bid = fresh_s(blockIdx.x), xcd = bid & 7, loc = bid >> 3;
  const int kh = xcd & 1, rt = (xcd >> 1) * 4 + (loc & 3), ct = loc >> 2;
  const int rot = ROT_K4 ? (ct + 2 * (loc & 3)) % 7 : 0;
  const rsrc_t rA = mk_rsrc(P.ws, WS_BYTES), rW = mk_rsrc(P.wd, F_ * D_ * 2);
  const int r = lane >> 3, sl8 = lane & 7, f = ((r >> 1) & 1) | ((j & 1) << 1);
  const unsigned voffW = r * 2048 + ((sl8 ^ (2 * f)) * 16), voffL = lane * 16;
  auto stX = [&](int kt, int sl) {
    const unsigned sa = WS_H + (unsigned)((rt * 8 + 2 * j) * 114688 + kt * 2048);
#pragma unroll
    for (int u = 0; u < 4; ++u) dma16c(rA, lds + sl * K4_STAGE + (4 * j + u) * 1024, voffL, sa + (u >> 1) * 114688 + (u & 1) * 1024);
  };
  auto stW = [&](int kt, int sl) {
    const unsigned sw = (unsigned)((kt * 64 + j * 8) * 2048 + ct * 256);
#pragma unroll
    for (int u = 0; u < 4; ++u) dma16(rW, lds + sl * K4_STAGE + 16384 + (u >> 1) * 8192 + (j + 4 * (u & 1)) * 1024, voffW, sw + (u >> 1) * 128 + (u & 1) * 32 * 2048);
  };
  const int k0 = kh * K4_NT + rot, k1 = kh * K4_NT + (rot + 1) % 7, k2 = kh * K4_NT + (rot + 2) % 7;
  stW(k0, 0); stW(k1, 1); stW(k2, 2);
  l_wait(P.ws, 64 * l + C_H + (rt >> 1) * 2 + kh, 16, lds, 5 * l + 4, j);
  stX(k0, 0); stX(k1, 1); stX(k2, 2);
  int w0 = kh * K4_NT, wr_ = (rot + 3) % 7, sl = 3;
  auto next = [&]() { const int kt = w0 + wr_; stX(kt, sl); stW(kt, sl); wr_ = wr_ == 6 ? 0 : wr_ + 1; if (wr_ == rot) w0 += 7; sl = (sl + 1) & 3; };
  WAITV(8); BAR(); next();
  WAITV(12); BAR(); next();
  for (int t = 2; t < K4_NT - 3; ++t) { WAITV(16); BAR(); next(); }
  drain_bar<2, 8>();
  BAR();
}
DI void p4_comp(const Params& P, int l, LAS char* lds, int wid) {
  STAMP_DECL STAMP_AT(0);
  if (wid >= 4) {
#pragma unroll 1
    for (int i = 0; i < K4_NT + 1; ++i) BAR();
    return;
  }
  const int lane = fresh_lane();
  const int bid = fresh_s(blockIdx.x), xcd = bid & 7, loc = bid >> 3;
  const int kh = xcd & 1, rt = (xcd >> 1) * 4 + (loc & 3), ct = loc >> 2;
  const int wr = wid >> 1, wc = wid & 1;
  const int m = lane & 15, g = lane >> 4, q = (lane >> 2) & 3, p = lane & 3;
  const int row0 = rt * 128 + wr * 64 + kh * 32 + m;
  const unsigned off0 = (unsigned)(row0 * D_ + ct * 128 + wc * 64 + 8 * g) * 2;
  int wa[4];
  { const int f = ((q >> 1) & 1) | ((g & 1) << 1);
#pragma unroll
    for (int nb = 0; nb < 4; ++nb) wa[nb] = 16384 + wc * 8192 + (8 * g + q) * 128 + (((4 * (nb >> 1) + p) ^ (2 * f)) * 16) + (((nb & 1) ^ (p >> 1)) * 8); }
  const int xa0 = wr * 8192 + lane * 16 + kh * 4096, xa1 = wr * 8192 + lane * 16 + 4096 - kh * 4096;
  f32x4 acc[4][4];
#pragma unroll
  for (int a = 0; a < 4; ++a)
#pragma unroll
    for (int b = 0; b < 4; ++b) acc[a][b] = (f32x4){0.f, 0.f, 0.f, 0.f};
  bf16x8 xf[2][4], wf[2][4];
  auto fetch = [&](int s, int sl) {
    LAS char* b = lds + sl * K4_STAGE;
    xf[s][0] = ld128(b + xa0 + s * 1024); xf[s][1] = ld128(b + xa0 + s * 1024 + 2048);
    xf[s][2] = ld128(b + xa1 + s * 1024); xf[s][3] = ld128(b + xa1 + s * 1024 + 2048);
#pragma unroll
    for (int nb = 0; nb < 4; ++nb) wf[s][nb] = ldtr(b + wa[nb] + s * 4096, 512);
  };
  auto mma = [&](int s) {
#pragma unroll
    for (int nb = 0; nb < 4; ++nb)
#pragma unroll
      for (int mb = 0; mb < 4; ++mb) acc[mb][nb] = mfma(wf[s][nb], xf[s][mb], acc[mb][nb]);
  };
#define K4_MIX() do { _Pragma("unroll") for (int i_ = 0; i_ < 12; ++i_) { SGB(0x8, 1); SGB(0x100, 1); } SGB(0x8, 4); SB0(); } while (0)
  const brs_t rws = mk_brs(P.ws, WS_BYTES);
  BAR();
  STAMP_AT(1);
  fetch(0, 0);
  SB0();
  int sl = 0;
  for (int t = 0; t < K4_NT - 1; ++t) {
    fetch(1, sl); mma(0); K4_MIX();
    sl = (sl + 1) & (K4_NS - 1);
    WAITL0(); BAR();
    fetch(0, sl); mma(1); K4_MIX();
  }
  fetch(1, sl); mma(0); K4_MIX();
  mma(1);
  STAMP_AT(2);
  const int pair = rt * 8 + ct;
  u32x4 res[2][2];
#pragma unroll
  for (int mb = 0; mb < 2; ++mb) { res[mb][0] = ldc16(rws, off0, WS_X1 + mb * 32768); res[mb][1] = ldc16(rws, off0, WS_X1 + mb * 32768 + 64); }
  FENCE();
#pragma unroll
  for (int i = 0; i < 4; ++i) { const f32x4 a = acc[2 + (i >> 1)][2 * (i & 1)], b = acc[2 + (i >> 1)][2 * (i & 1) + 1];
    const u32x4 o = {pk2(a[0], a[1]), pk2(a[2], a[3]), pk2(b[0], b[1]), pk2(b[2], b[3])};
    stc16(rws, lane * 16, WS_XCH + pair * 65536 + ((kh ^ 1) * 4 + wid) * 8192 + i * 1024, o); }
#pragma unroll
  for (int mb = 0; mb < 2; ++mb) { res_unpack(acc[mb], res[mb], g >= 2, true); PIN(acc[mb][0]); PIN(acc[mb][1]); PIN(acc[mb][2]); PIN(acc[mb][3]); }
  WAITV(0);
  volatile LAS unsigned* relay = (volatile LAS unsigned*)(lds + L_SYNC + 48);
  { unsigned old = 0;
    if (lane == 0) old = __hip_atomic_fetch_add((LAS unsigned*)(lds + L_SYNC + 52), 1u, __ATOMIC_RELAXED, __HIP_MEMORY_SCOPE_WORKGROUP);
    old = __builtin_amdgcn_readfirstlane(old);
    if ((old & 3) == 3) {
      if (lane == 0) __hip_atomic_store(cnt_ptr(P.ws, C_FLAG + 256 * l + pair * 2 + kh), 1u, __ATOMIC_RELAXED, __HIP_MEMORY_SCOPE_AGENT);
      poll_ge(cnt_ptr(P.ws, C_FLAG + 256 * l + pair * 2 + (kh ^ 1)), 1);
      *relay = l + 1;
    } else { unsigned spins = 0; while ((unsigned)__builtin_amdgcn_readfirstlane(*relay) < (unsigned)(l + 1) && ++spins < (1u << 24)) __builtin_amdgcn_s_sleep(1); }
    FENCE(); }
  u32x4 in[8];
#pragma unroll
  for (int i = 0; i < 4; ++i) in[i] = ldc16(rws, lane * 16, WS_XCH + pair * 65536 + (kh * 4 + wid) * 8192 + i * 1024);
  BAR();
#pragma unroll
  for (int i = 0; i < 4; ++i) { const f32x4 a = {bflo(in[i][0]), bfhi(in[i][0]), bflo(in[i][1]), bfhi(in[i][1])}, b = {bflo(in[i][2]), bfhi(in[i][2]), bflo(in[i][3]), bfhi(in[i][3])};
    acc[i >> 1][2 * (i & 1)] += a; acc[i >> 1][2 * (i & 1) + 1] += b; }
  const brs_t rxo = mk_brs(P.out, S_ * D_ * 2);
#pragma unroll
  for (int mb = 0; mb < 2; ++mb)
    x_store(acc[mb], rxo, off0 + xs_adj(m) + mb * 32768, rws, WS_ST + (unsigned)((2 * l + 2) & 3) * 262144u + ((ct * 2 + wc) * 2048 + row0 + mb * 16) * 8, m, g, l == 0);
  if (l == 0) c_signal<true>(P.ws, 64 + C_X + rt, lds, 4, 4);
  else if (bid == 0 && wid == 0) { const u64 tag = call_tag(P.ws); if (lane == 0) __hip_atomic_store((u64*)(P.ws + WS_EPOCH), tag, __ATOMIC_RELAXED, __HIP_MEMORY_SCOPE_AGENT); }
  STAMP_END(5 + 5 * l);
}

DI void init_sync(LAS char* lds) { if (threadIdx.x < 32) *(LAS unsigned*)(lds + L_SYNC + threadIdx.x * 4) = 0u; WAITL0(); }

__global__ __launch_bounds__(768, 3) void k_mega(Params P) {
  extern __shared__ __attribute__((aligned(16))) char smem[];
  LAS char* lds = (LAS char*)smem;
  const int wid = __builtin_amdgcn_readfirstlane(threadIdx.x >> 6);
  if (wid >= 8) {
    const int j = wid & 3;
#pragma unroll 1
    for (int l = 0; l < 2; ++l) { p1_load(fresh_params(), fresh_s(l), lds, fresh_s(j)); p2_load<true>(fresh_params(), fresh_s(l), lds, fresh_s(j)); p3_load(fresh_params(), fresh_s(l), lds, fresh_s(j)); p4_load(fresh_params(), fresh_s(l), lds, fresh_s(j)); }
  } else {
    init_sync(lds);
#pragma unroll 1
    for (int l = 0; l < 2; ++l) {
      p1_comp(fresh_params(), fresh_s(l), lds, fresh_s(wid)); p2_comp(fresh_params(), fresh_s(l), lds, fresh_s(wid));
      p3_comp(fresh_params(), fresh_s(l), lds, fresh_s(wid)); p4_comp(fresh_params(), fresh_s(l), lds, fresh_s(wid));
    }
  }
}

extern "C" void kernel_launch(void* const* d_in, const int* in_sizes, int n_in, void* d_out, int out_size, void* d_ws, size_t ws_size, hipStream_t stream) {
  Params P;
  P.x0 = (const u16*)d_in[0]; P.wqkv = (const u16*)d_in[1]; P.wo = (const u16*)d_in[2];
  P.wg = (const u16*)d_in[4]; P.wu = (const u16*)d_in[5]; P.wd = (const u16*)d_in[6];
  P.out = (u16*)d_out; P.ws = (char*)d_ws;
  (void)hipFuncSetAttribute((const void*)k_mega, hipFuncAttributeMaxDynamicSharedMemorySize, L_BYTES);
  k_mega<<<256, 768, L_BYTES, stream>>>(P);
}
```

```cpp
#include <hip/hip_runtime.h>
#include <hip/hip_bf16.h>
#include <stdint.h>

#define LAS __attribute__((address_space(3)))
#define DI __device__ __forceinline__
typedef unsigned short u16;
typedef __bf16 bf16x8 __attribute__((ext_vector_type(8)));
typedef __bf16 bf16x2 __attribute__((ext_vector_type(2)));
typedef short s16x4 __attribute__((ext_vector_type(4)));
typedef float f32x2 __attribute__((ext_vector_type(2)));
typedef float f32x4 __attribute__((ext_vector_type(4)));
typedef unsigned u32x2 __attribute__((ext_vector_type(2)));
typedef unsigned u32x4 __attribute__((ext_vector_type(4)));
typedef u32x4 rsrc_t;
typedef __amdgpu_buffer_rsrc_t brs_t;

constexpr int S_ = 2048, D_ = 1024, F_ = 3584;
#define MEGA 1
#define QDEF 7
#define SNAKE 1
#define P4_LATEBAR 1
#define HELP_PRIO 2
#define K1_EARLYW 0
#define REP_XCD 0
#define FINE3 1
#define FINE4 1
#define FINE1 1
#define X_FULL 1
#define PF_ON 1
#define POLL_SLEEP 1
#define ROT_K2 0
#define ROT_K4 1
constexpr unsigned OOB_V = 0x80000000u;
constexpr unsigned WS_PQ = 0, WS_KVP = 4u << 20, WS_X1 = 8u << 20, WS_H = 12u << 20, WS_XCH = 27u << 20, WS_ST = 35u << 20, WS_CS = 36u << 20, WS_CNT = 37u << 20,
                   WS_BYTES = 38u << 20;
#define NREP 8
constexpr int C_X = 0, C_KV = 16, C_O = 32, C_X1 = 48, C_H = 56, F_X1 = 72, F_H = 80, F_X = 96, C_FLAG = 256 * NREP, C_N = C_FLAG + 512;
constexpr unsigned WS_TAG = WS_CNT + C_N * 128, T_X0 = 0, T_CS = 16, WS_EPOCH = WS_TAG + 32 * 128;
static_assert(C_N == 10 * 256 && NREP == 8, "counter zeroing map");
constexpr int L_STATS = 147456, L_CS = L_STATS + 2048, L_CS1 = L_CS + 1024, L_KV = L_CS1 + 1024, L_SYNC = L_KV + 4224, L_BYTES = L_SYNC + 128;

struct Params { const u16* x0; const u16* wqkv; const u16* wo; const u16* wg; const u16* wu; const u16* wd; u16* out; char* ws; };

typedef const __attribute__((address_space(4))) Params* KP;
DI Params fresh_params() {
  KP p = (KP)__builtin_amdgcn_kernarg_segment_ptr(); asm volatile("" : "+s"(p));
  Params r; r.x0 = p->x0; r.wqkv = p->wqkv; r.wo = p->wo; r.wg = p->wg; r.wu = p->wu; r.wd = p->wd; r.out = p->out; r.ws = p->ws; return r;
}
DI rsrc_t mk_rsrc(const void* p, unsigned bytes) {
  const unsigned long long a = (unsigned long long)p;
  rsrc_t r = {(unsigned)a, (unsigned)(a >> 32) & 0xffffu, bytes, 0x00020000u};
  return r;
}
DI brs_t mk_brs(const void* p, unsigned bytes) { return __builtin_amdgcn_make_buffer_rsrc((void*)p, 0, bytes, 0x00020000); }
DI void dma16(rsrc_t r, LAS char* dst, unsigned voff, unsigned soff) {
  asm volatile("s_mov_b32 m0, %0\n\ts_nop 0\n\tbuffer_load_dwordx4 %1, %2, %3 offen lds" :: "s"((unsigned)(size_t)dst), "v"(voff), "s"(r), "s"(soff) : "memory");
}
DI void dma16c(rsrc_t r, LAS char* dst, unsigned voff, unsigned soff) {
  asm volatile("s_mov_b32 m0, %0\n\ts_nop 0\n\tbuffer_load_dwordx4 %1, %2, %3 offen sc1 lds" :: "s"((unsigned)(size_t)dst), "v"(voff), "s"(r), "s"(soff) : "memory");
}
DI u32x4 ldc16(brs_t r, unsigned voff, unsigned soff) { return __builtin_amdgcn_raw_buffer_load_b128(r, voff, soff, 16); }
DI void stc16(brs_t r, unsigned voff, unsigned soff, u32x4 v) { __builtin_amdgcn_raw_buffer_store_b128(v, r, voff, soff, 16); }
DI void stc16(brs_t r, unsigned voff, unsigned soff, f32x4 v) { stc16(r, voff, soff, __builtin_bit_cast(u32x4, v)); }
DI void stc8(brs_t r, unsigned voff, unsigned soff, u32x2 v) { __builtin_amdgcn_raw_buffer_store_b64(v, r, voff, soff, 16); }
DI f32x4 mfma(bf16x8 a, bf16x8 b, f32x4 c) { return __builtin_amdgcn_mfma_f32_16x16x32_bf16(a, b, c, 0, 0, 0); }
DI bf16x8 ld128(LAS char* p) { return *(LAS bf16x8*)p; }
DI bf16x8 ldtr(LAS char* p, int off2) {
  s16x4 a = __builtin_amdgcn_ds_read_tr16_b64_v4i16((LAS s16x4*)p);
  s16x4 b = __builtin_amdgcn_ds_read_tr16_b64_v4i16((LAS s16x4*)(p + off2));
  return __builtin_bit_cast(bf16x8, __builtin_shufflevector(a, b, 0, 1, 2, 3, 4, 5, 6, 7));
}
DI unsigned pk2(float a, float b) { f32x2 v = {a, b}; return __builtin_bit_cast(unsigned, __builtin_convertvector(v, bf16x2)); }
DI float bflo(unsigned u) { return __uint_as_float(u << 16); }
DI float bfhi(unsigned u) { return __uint_as_float(u & 0xffff0000u); }
DI unsigned phi_ln2(f32x2 u, f32x2 rs, f32x2 rs2) {
  const f32x2 eps = {1e-6f, 1e-6f}, a = __builtin_elementwise_fma(u, rs, eps), t = u * rs2;
  f32x2 e, mx;
  asm("v_exp_f32 %0, %1 clamp" : "=v"(e[0]) : "v"(t[0])); asm("v_exp_f32 %0, %1 clamp" : "=v"(e[1]) : "v"(t[1]));
  mx[0] = fmaxf(a[0], 1e-6f); mx[1] = fmaxf(a[1], 1e-6f);
  return __builtin_bit_cast(unsigned, __builtin_convertvector(mx + e, bf16x2));
}
DI f32x2 lo2(f32x4 v) { return __builtin_shufflevector(v, v, 0, 1); }
DI f32x2 hi2(f32x4 v) { return __builtin_shufflevector(v, v, 2, 3); }
DI float xsum(float s) {
  const auto a = __builtin_amdgcn_permlane16_swap(__float_as_uint(s), __float_as_uint(s), false, false);
  s = __uint_as_float(a[0]) + __uint_as_float(a[1]);
  const auto b = __builtin_amdgcn_permlane32_swap(__float_as_uint(s), __float_as_uint(s), false, false);
  return __uint_as_float(b[0]) + __uint_as_float(b[1]);
}
#define SGB(mask, n) __builtin_amdgcn_sched_group_barrier(mask, n, 0)
#define SB0() __builtin_amdgcn_sched_barrier(0)
#define PIN(x) asm volatile("" : "+v"(x))
#define FENCE() asm volatile("" ::: "memory")
#define BAR() do { FENCE(); __builtin_amdgcn_s_barrier(); FENCE(); __builtin_amdgcn_sched_barrier(0); } while (0)
#define WAITV(n) asm volatile("s_waitcnt vmcnt(%0)" :: "i"(n) : "memory")
#define WAITL0() do { FENCE(); __builtin_amdgcn_s_waitcnt(0xc07f); FENCE(); } while (0)
DI int fresh_s(int x) { asm volatile("" : "+s"(x)); return x; }
DI int fresh_lane() { int t; asm volatile("v_mbcnt_lo_u32_b32 %0, -1, 0\n\tv_mbcnt_hi_u32_b32 %0, -1, %0" : "=v"(t)); return t; }

#define STAMP_DECL
#define STAMP_AT(i)
#define STAMP_END(sl_)

template <int N> DI void pf_lines(const void* p) {
#pragma unroll
  for (int i = 0; i < 2 * N; ++i) asm volatile("s_load_dword s100, %0, %1" :: "s"(p), "i"(i * 64) : "s100");
}
DI unsigned* cnt_ptr(char* ws, int idx) { return (unsigned*)(ws + WS_CNT + idx * 128); }
DI unsigned* rep_ptr(char* ws, int idx) { return cnt_ptr(ws, idx * NREP + (REP_XCD ? (blockIdx.x & 7) : ((blockIdx.x >> 3) + (blockIdx.x & 7) * 5) % NREP)); }
DI void poll_ge(unsigned* c, unsigned target) {
  unsigned spins = 0;
  while ((unsigned)__builtin_amdgcn_readfirstlane(__hip_atomic_load(c, __ATOMIC_RELAXED, __HIP_MEMORY_SCOPE_AGENT)) < target && ++spins < (1u << 21)) __builtin_amdgcn_s_sleep(POLL_SLEEP);
  FENCE();
}
DI void l_wait(char* ws, int idx, unsigned target, LAS char* lds, unsigned epoch, int j) {
  if (!MEGA) return;
  volatile LAS unsigned* relay = (volatile LAS unsigned*)(lds + L_SYNC);
  if (j == 0) { poll_ge(rep_ptr(ws, idx), target); *relay = epoch; }
  else { unsigned spins = 0; while ((unsigned)__builtin_amdgcn_readfirstlane(*relay) < epoch && ++spins < (1u << 24)) __builtin_amdgcn_s_sleep(1); }
  FENCE();
}
#define LOOK(v, ptr) asm volatile("global_load_dword %0, %1, %2 sc1" : "=v"(v) : "v"(0), "s"(ptr) : "memory")
#define LOOK_CHECK(v, ptr, target, N) do { asm volatile("s_waitcnt vmcnt(%1)" : "+v"(v) : "i"(N) : "memory"); if ((unsigned)__builtin_amdgcn_readfirstlane(v) < (target)) poll_ge(ptr, target); } while (0)
template <bool REP> DI void c_signal(char* ws, int idx, LAS char* lds, int arr, unsigned nw, int idx2 = -1) {
  if (!MEGA) return;
  WAITV(0);
  const int lane = fresh_lane();
  unsigned old = 0;
  if (lane == 0) old = __hip_atomic_fetch_add((LAS unsigned*)(lds + L_SYNC + 4 + arr * 4), 1u, __ATOMIC_RELAXED, __HIP_MEMORY_SCOPE_WORKGROUP);
  if ((unsigned)__builtin_amdgcn_readfirstlane(old) == nw - 1 && lane < (idx2 < 0 ? NREP : 2 * NREP))
    __hip_atomic_fetch_add(cnt_ptr(ws, lane < NREP ? idx * NREP + lane : (idx2 - 1) * NREP + lane), 1u, __ATOMIC_RELAXED, __HIP_MEMORY_SCOPE_AGENT);
}

typedef unsigned long long u64;
DI u64 call_tag(const char* ws) {
  const u64 e = __hip_atomic_load((const u64*)(ws + WS_EPOCH), __ATOMIC_RELAXED, __HIP_MEMORY_SCOPE_AGENT);
  u64 z = ((u64)(unsigned)__builtin_amdgcn_readfirstlane((unsigned)(e >> 32)) << 32 | (unsigned)__builtin_amdgcn_readfirstlane((unsigned)e)) + 0x9E3779B97F4A7C15ull;
  z = (z ^ (z >> 30)) * 0xBF58476D1CE4E5B9ull; z = (z ^ (z >> 27)) * 0x94D049BB133111EBull;
  return z ^ (z >> 31);
}
DI u64* tag_ptr(char* ws, int blk) { return (u64*)(ws + WS_TAG + blk * 128); }
DI void tag_poll(u64* f, int np, u64 tag) {
  const int lane = fresh_lane();
  unsigned spins = 0;
  while (!__all(__hip_atomic_load(f + (lane < np ? lane : 0), __ATOMIC_RELAXED, __HIP_MEMORY_SCOPE_AGENT) == tag) && ++spins < (1u << 21)) __builtin_amdgcn_s_sleep(2);
  FENCE();
}
template <int NT, bool PAIR> DI f32x2 combine_stats(const u32x2 (&u)[NT]) {
  float s = 0.f;
#pragma unroll
  for (int i = 0; i < NT; ++i) s += __uint_as_float(u[i][0]);
  if (PAIR) s += __shfl_xor(s, 1);
  const float mean = s * (1.f / 1024.f);
  float q = 0.f;
#pragma unroll
  for (int i = 0; i < NT; ++i) { const float d = __uint_as_float(u[i][0]) * (1.f / 64.f) - mean; q += __uint_as_float(u[i][1]) + 64.f * d * d; }
  if (PAIR) q += __shfl_xor(q, 1);
  f32x2 r; r[0] = mean; r[1] = __builtin_amdgcn_rsqf(q * (1.f / 1024.f) + 1e-5f);
  return r;
}
DI u32x4 ones_bf16() { unsigned o = 0x3f803f80u; asm volatile("" : "+v"(o)); const u32x4 r = {o, o, o, o}; return r; }
DI void res_unpack(f32x4 (&a)[4], const u32x4 (&res)[2], bool hi, bool add) {
#pragma unroll
  for (int pr = 0; pr < 2; ++pr) {
    const unsigned u0 = hi ? res[pr][2] : res[pr][0], u1 = hi ? res[pr][3] : res[pr][1], u2 = hi ? res[pr][0] : res[pr][2], u3 = hi ? res[pr][1] : res[pr][3];
    const f32x4 x = {bflo(u0), bfhi(u0), bflo(u1), bfhi(u1)}, y = {bflo(u2), bfhi(u2), bflo(u3), bfhi(u3)};
    if (add) { a[2 * pr] += x; a[2 * pr + 1] += y; } else { a[2 * pr] = x; a[2 * pr + 1] = y; }
  }
}
DI void x_store(const f32x4 (&a)[4], brs_t rxo, unsigned xoff, brs_t rws, unsigned stoff, int m, int g, bool stats) {
  const bool hi = g >= 2;
  const u32x4 ones4 = ones_bf16();
  f32x4 sm = {0.f, 0.f, 0.f, 0.f}, sq = {0.f, 0.f, 0.f, 0.f};
  u32x4 o[2];
#pragma unroll
  for (int pr = 0; pr < 2; ++pr) {
    const unsigned p0 = pk2(a[2 * pr][0], a[2 * pr][1]), p1 = pk2(a[2 * pr][2], a[2 * pr][3]), p2 = pk2(a[2 * pr + 1][0], a[2 * pr + 1][1]), p3 = pk2(a[2 * pr + 1][2], a[2 * pr + 1][3]);
    o[pr][0] = hi ? p2 : p0; o[pr][1] = hi ? p3 : p1; o[pr][2] = hi ? p0 : p2; o[pr][3] = hi ? p1 : p3;
  }
  u32x4 sa, sb;
#pragma unroll
  for (int e = 0; e < 4; ++e) {
    sa[e] = __builtin_amdgcn_update_dpp(o[0][e], o[1][e], 0x128, 0xf, 0xc, false);
    sb[e] = __builtin_amdgcn_update_dpp(o[1][e], o[0][e], 0x128, 0xf, 0x3, false);
  }
  stc16(rxo, xoff, 0, sa); stc16(rxo, xoff, 8 * D_ * 2, sb);
  if (!stats) return;
#pragma unroll
  for (int pr = 0; pr < 2; ++pr) {
    const bf16x8 f = __builtin_bit_cast(bf16x8, o[pr]);
    sm = mfma(__builtin_bit_cast(bf16x8, ones4), f, sm);
    sq = mfma(f, f, sq);
  }
  const float dq = (m & 2) ? ((m & 1) ? sq[3] : sq[2]) : ((m & 1) ? sq[1] : sq[0]);
  if (g == (m >> 2)) { const u32x2 r = {__float_as_uint(sm[0]), __float_as_uint(fmaxf(dq - sm[0] * sm[0] * (1.f / 64.f), 0.f))}; stc8(rws, stoff, 0, r); }
}
DI unsigned xs_adj(int m) { return X_FULL ? (unsigned)((m >> 3) * (64 - 8 * D_ * 2)) : 0u; }
template <int N, int NI> DI void drain_bar() { WAITV(N * NI); BAR(); if constexpr (N > 0) drain_bar<N - 1, NI>(); }

#define STAMP_WID wid
#define ALOAD8C(dst, rs, voff, soff) asm volatile("buffer_load_dwordx2 %0, %1, %2, %3 offen sc1" : "=v"(dst) : "v"(voff), "s"(rs), "s"(soff) : "memory")

constexpr int K1_STAGE = 40960, K1_RED = 3 * K1_STAGE, K1_D = QDEF;
DI void p1_load(const Params& P, int l, LAS char* lds, int j) {
  const int lane = fresh_lane();
  const int bid = fresh_s(blockIdx.x), xcd = bid & 7, loc = bid >> 3;
  const int rt = (xcd & 1) * 8 + (loc & 7), hp = (xcd >> 1) * 4 + (loc >> 3);
  const int rot = l ? 0 : hp + rt;
  const rsrc_t rX = mk_rsrc(l ? P.out : P.x0, S_ * D_ * 2), rW = mk_rsrc(P.wqkv, D_ * 3072 * 2);
  const int r = lane >> 3, sl8 = lane & 7, f = ((r >> 1) & 1) | ((j & 1) << 1);
  const unsigned voffX = r * 2048 + ((sl8 ^ (r & 6)) * 16), voffW = r * 6144 + ((sl8 ^ (2 * f)) * 16);
  auto stX = [&](int t, int st) {
    const unsigned sx = (unsigned)((rt * 128 + j * 32) * 2048 + ((t + rot) & 15) * 128);
#pragma unroll
    for (int u = 0; u < 4; ++u) dma16c(rX, lds + st * K1_STAGE + (4 * j + u) * 1024, voffX, sx + u * 16384);
  };
  auto stW = [&](int t, int st, int p0 = 0, int p1 = 3) {
    const unsigned sw = (unsigned)((((t + rot) & 15) * 64 + j * 8) * 6144 + hp * 128);
#pragma unroll
    for (int s = 0; s < 2; ++s)
#pragma unroll
      for (int pnl = p0; pnl < p1; ++pnl) dma16(rW, lds + st * K1_STAGE + 16384 + pnl * 8192 + (4 * s + j) * 1024, voffW, sw + pnl * 2048 + s * 32 * 6144);
  };
  if (l) { stW(0, 0); stW(1, 1); l_wait(P.ws, FINE1 ? 128 + F_X + rt : 128 + C_X + rt, FINE1 ? 4 : 16, lds, 5, j); stX(0, 0); stX(1, 1); WAITV(4); }
  else { stX(0, 0); stW(0, 0); stX(1, 1); stW(1, 1); WAITV(10); }
  BAR();
  unsigned look = 0;
  if (l && j == 0) LOOK(look, rep_ptr(P.ws, 128 + C_X + rt));
  stX(2, 2); stW(2, 2);
  WAITV(10); BAR();
  stX(3, 0); stW(3, 0);
  if (l && j == 0) LOOK_CHECK(look, rep_ptr(P.ws, 128 + C_X + rt), 16u, 20);
  WAITV(10); BAR();
  stX(4, 1); stW(4, 1);
  WAITV(10); BAR();
  int st = 2;
  for (int t = 3; t < 14 - K1_D; ++t) {
    stX(t + 2, st); stW(t + 2, st);
    if (l == 0) pf_lines<4>((const char*)P.wo + ((bid * 4 + j) * 16 + ((t - 3) & 3) * 4) * 128);
    WAITV(10); BAR();
    st = st == 2 ? 0 : st + 1;
  }
  for (int t = 14 - K1_D; t < 14; ++t) {
    stX(t + 2, st); stW(t + 2, st, 1, 3);
    WAITV(8); BAR();
    st = st == 2 ? 0 : st + 1;
  }
  for (int t = 14; t < 14 + K1_D; ++t) {
    stX(t + 2 - K1_D, st); stW(t + 2 - K1_D, st, 0, 1);
    if (t == 16) BAR();
    WAITV(6); BAR();
    st = st == 2 ? 0 : st + 1;
  }
  WAITV(0); BAR();
  BAR(); BAR();
}
DI void p1_comp(const Params& P, int l, LAS char* lds, int wid) {
  STAMP_DECL STAMP_AT(0);
  const int bid = fresh_s(blockIdx.x), xcd = bid & 7, loc = bid >> 3;
  const int rt = (xcd & 1) * 8 + (loc & 7), hp = (xcd >> 1) * 4 + (loc >> 3);
  if (wid >= 4) {
    {
      const int lane = fresh_lane(), i4 = wid - 4, tid = i4 * 64 + lane;
      const brs_t rws = mk_brs(P.ws, WS_BYTES);
      int nb = 0;
      __builtin_amdgcn_s_setprio(HELP_PRIO);
      if (l == 0) {
        const u64 tag = call_tag(P.ws);
        if (wid == 4 && lane < 11) { const int w = lane < 10 ? bid * 10 + lane : (C_KV + hp) * NREP + (rt & (NREP - 1));
          if (lane == 10 || w < C_KV * NREP || w >= C_O * NREP) __hip_atomic_store(cnt_ptr(P.ws, w), 0u, __ATOMIC_RELAXED, __HIP_MEMORY_SCOPE_AGENT); }
        const int m = lane & 15, g = lane >> 4, q = (lane >> 2) & 3, p = lane & 3, kt = (hp + rt) & 15;
        const int xa0 = (i4 * 32 + m) * 128 + ((g ^ (m & 6)) * 16), xa1 = xa0 ^ 64;
        const int wa = 16384 + (8 * g + q) * 128 + (((2 * i4 + (p >> 1)) ^ (2 * (((q >> 1) & 1) | ((g & 1) << 1)))) * 16) + (p & 1) * 8;
        const bf16x8 ones = __builtin_bit_cast(bf16x8, ones_bf16());
        const f32x4 z4 = {0.f, 0.f, 0.f, 0.f};
        BAR();
#pragma unroll
        for (int mb = 0; mb < 2; ++mb) {
          const bf16x8 x0 = ld128(lds + xa0 + mb * 2048), x1 = ld128(lds + xa1 + mb * 2048);
          const f32x4 sm = mfma(ones, x1, mfma(ones, x0, z4)), sq = mfma(x1, x1, mfma(x0, x0, z4));
          const float dq = (m & 2) ? ((m & 1) ? sq[3] : sq[2]) : ((m & 1) ? sq[1] : sq[0]);
          if (g == (m >> 2)) { const u32x2 r = {__float_as_uint(sm[0]), __float_as_uint(fmaxf(dq - sm[0] * sm[0] * (1.f / 64.f), 0.f))}; stc8(rws, m * 8, WS_ST + (kt * 2048 + rt * 128 + i4 * 32 + mb * 16) * 8, r); }
        }
#pragma unroll
        for (int pnl = 0; pnl < 3; ++pnl) {
          const f32x4 c = mfma(ones, ldtr(lds + wa + pnl * 8192 + 4096, 512), mfma(ones, ldtr(lds + wa + pnl * 8192, 512), z4));
          if (lane < 16) __builtin_amdgcn_raw_buffer_store_b32(__float_as_uint(c[0]), rws, lane * 4, WS_CS + (kt * 3072 + pnl * 1024 + hp * 64 + i4 * 16) * 4, 16);
        }
        BAR(); BAR();
        WAITV(0);
        { unsigned old = 0;
          if (lane == 0) old = __hip_atomic_fetch_add((LAS unsigned*)(lds + L_SYNC + 64), 1u, __ATOMIC_RELAXED, __HIP_MEMORY_SCOPE_WORKGROUP);
          if (__builtin_amdgcn_readfirstlane(old) == 3 && lane < 2)
            __hip_atomic_store(lane ? tag_ptr(P.ws, T_CS + hp) + rt : tag_ptr(P.ws, T_X0 + rt) + hp, tag, __ATOMIC_RELAXED, __HIP_MEMORY_SCOPE_AGENT); }
#pragma unroll 1
        for (int i = 3; i < 9; ++i) BAR();
        if (wid < 6) tag_poll(tag_ptr(P.ws, wid == 4 ? T_X0 + rt : T_CS + hp), 16, tag);
        BAR();
        nb = 10;
      } else { BAR(); BAR(); BAR(); nb = 3; }
      u32x2 sv[8]; float cv[16];
#pragma unroll
      for (int i = 0; i < 8; ++i) sv[i] = __builtin_amdgcn_raw_buffer_load_b64(rws, (tid & 1) * 131072 + (tid >> 1) * 8, WS_ST + (unsigned)(2 * l) * 262144u + rt * 1024 + i * 16384, 16);
      if (l == 0) { const int ci = tid < 192 ? tid : 0;
#pragma unroll
        for (int i = 0; i < 16; ++i) cv[i] = __uint_as_float(__builtin_amdgcn_raw_buffer_load_b32(rws, ((ci >> 6) * 1024 + (ci & 63)) * 4, WS_CS + (i * 3072 + hp * 64) * 4, 16)); }
      BAR(); BAR();
      const f32x2 mr = combine_stats<8, true>(sv);
      if (!(tid & 1)) { LAS float* sp = (LAS float*)(lds + L_STATS) + (tid >> 1); sp[0] = mr[0]; sp[128] = mr[1]; sp[256] = mr[1] * 1.44269504f; }
      if (l == 0) { float c = 0.f;
#pragma unroll
        for (int i = 0; i < 16; ++i) c += cv[i];
        if (tid < 192) *(LAS float*)(lds + L_CS1 + tid * 4) = c; }
      WAITL0();
      __builtin_amdgcn_s_setprio(0);
#pragma unroll 1
      for (int i = nb + 2; i < 16 + K1_D; ++i) BAR();
    }
    unsigned look = 0;
    if (wid == 4) LOOK(look, rep_ptr(P.ws, 128 * l + C_KV + hp));
    BAR();
    STAMP_AT(0);
    volatile LAS unsigned* relay = (volatile LAS unsigned*)(lds + L_SYNC + 56);
    if (wid == 4) {
      asm volatile("s_waitcnt vmcnt(0)" : "+v"(look) :: "memory");
      if ((unsigned)__builtin_amdgcn_readfirstlane(look) < 16u) poll_ge(rep_ptr(P.ws, 128 * l + C_KV + hp), 16);
      *relay = l + 1;
    }
    else { unsigned spins = 0; while ((unsigned)__builtin_amdgcn_readfirstlane(*relay) < (unsigned)(l + 1) && ++spins < (1u << 24)) __builtin_amdgcn_s_sleep(1); }
    FENCE();
    STAMP_AT(1);
    const int lane = fresh_lane(), hw = (wid >> 1) & 1, wr = wid & 1;
    const brs_t rws = mk_brs(P.ws, WS_BYTES);
    const unsigned src = WS_KVP + l * (512 * 2176) + (2 * hp + hw) * 2176;
    u32x4 v[16];
#pragma unroll
    for (int i = 0; i < 16; ++i) v[i] = ldc16(rws, lane * 16, src + i * 32 * 2176 + wr * 1024);
    const u32x2 zv = __builtin_amdgcn_raw_buffer_load_b64(rws, (lane >> 2) * (32 * 2176) + (lane & 3) * 8, src + 2048 + wr * 32, 16);
    float s[8] = {0.f, 0.f, 0.f, 0.f, 0.f, 0.f, 0.f, 0.f};
#pragma unroll
    for (int i = 0; i < 16; ++i)
#pragma unroll
      for (int e = 0; e < 4; ++e) { s[2 * e] += bflo(v[i][e]); s[2 * e + 1] += bfhi(v[i][e]); }
#pragma unroll
    for (int b = 0; b < 2; ++b) {
      const int e = 16 * b + (lane & 15), i16 = (e >> 3) * 4 + (e & 3), bp = (e >> 2) & 1;
      const u32x2 o = {pk2(s[4 * b], s[4 * b + 1]), pk2(s[4 * b + 2], s[4 * b + 3])};
      *(LAS u32x2*)(lds + L_KV + hw * 2112 + bp * 1024 + ((lane >> 4) * 16 + i16) * 16 + 8 * wr) = o;
    }
    float z[4] = {bflo(zv[0]), bfhi(zv[0]), bflo(zv[1]), bfhi(zv[1])};
#pragma unroll
    for (int e = 0; e < 4; ++e) {
      z[e] += __uint_as_float(__builtin_amdgcn_update_dpp(0u, __float_as_uint(z[e]), 0x124, 0xf, 0xf, false));
      z[e] += __uint_as_float(__builtin_amdgcn_update_dpp(0u, __float_as_uint(z[e]), 0x128, 0xf, 0xf, false));
      z[e] = xsum(z[e]);
    }
    if (lane < 4) { const u32x2 oz = {pk2(z[0], z[1]), pk2(z[2], z[3])}; *(LAS u32x2*)(lds + L_KV + hw * 2112 + 2048 + lane * 16 + 8 * wr) = oz; }
    WAITL0();
    BAR(); BAR();
    return;
  }
  const int wr = wid >> 1, wc = wid & 1;
  f32x4 acc[3][4][2];
  {
    const int lane = fresh_lane(), m = lane & 15, g = lane >> 4, q = (lane >> 2) & 3, p = lane & 3;
    int xa0 = (wr * 64 + m) * 128 + ((g ^ (m & 6)) * 16), xa1 = xa0 ^ 64;
    int wa0 = 16384 + (8 * g + q) * 128 + (((4 * wc + (p >> 1)) ^ (2 * (((q >> 1) & 1) | ((g & 1) << 1)))) * 16) + (p & 1) * 8, wa1 = wa0 ^ 32;
#pragma unroll
    for (int a = 0; a < 3; ++a)
#pragma unroll
      for (int b = 0; b < 4; ++b)
#pragma unroll
        for (int c = 0; c < 2; ++c) acc[a][b][c] = (f32x4){0.f, 0.f, 0.f, 0.f};
    bf16x8 xf[4], wf[2][3][2];
    auto step = [&](int cur, bool q_now, bool q_next) {
      LAS char* xp = lds + (cur ? xa0 : xa1); LAS char* w0 = lds + wa0 + (cur ? 0 : 4096); LAS char* w1 = lds + wa1 + (cur ? 0 : 4096);
#pragma unroll
      for (int mb = 0; mb < 4; ++mb) {
        const bf16x8 x = xf[mb];
        if (q_next) {
          if (mb == 1) { wf[cur ^ 1][0][0] = ldtr(w0, 512); xf[0] = ld128(xp); wf[cur ^ 1][1][0] = ldtr(w0 + 8192, 512); }
          if (mb == 2) { wf[cur ^ 1][2][0] = ldtr(w0 + 16384, 512); xf[1] = ld128(xp + 2048); wf[cur ^ 1][0][1] = ldtr(w1, 512); }
          if (mb == 3) { wf[cur ^ 1][1][1] = ldtr(w1 + 8192, 512); xf[2] = ld128(xp + 4096); wf[cur ^ 1][2][1] = ldtr(w1 + 16384, 512); }
        } else {
          if (mb == 1) { wf[cur ^ 1][1][0] = ldtr(w0 + 8192, 512); wf[cur ^ 1][2][0] = ldtr(w0 + 16384, 512); }
          if (mb == 2) { xf[0] = ld128(xp); wf[cur ^ 1][1][1] = ldtr(w1 + 8192, 512); xf[1] = ld128(xp + 2048); }
          if (mb == 3) { wf[cur ^ 1][2][1] = ldtr(w1 + 16384, 512); xf[2] = ld128(xp + 4096); }
        }
#pragma unroll
        for (int nb = 0; nb < 2; ++nb) {
          if (q_now) acc[0][mb][nb] = mfma(wf[cur][0][nb], x, acc[0][mb][nb]);
          acc[1][mb][nb] = mfma(x, wf[cur][1][nb], acc[1][mb][nb]);
          acc[2][mb][nb] = mfma(x, wf[cur][2][nb], acc[2][mb][nb]);
        }
        if (mb == 0 && cur) { SB0(); WAITL0(); BAR(); }
        if (mb == 3) xf[3] = ld128(xp + 6144);
        if (mb > 0 || !cur) { _Pragma("unroll") for (int i_ = 0; i_ < 6; ++i_) { SGB(0x8, 1); SGB(0x100, 1); } }
        SB0();
      }
    };
    auto advance = [&](int& st) {
      const int d = st == 2 ? -2 * K1_STAGE : K1_STAGE; st = st == 2 ? 0 : st + 1;
      xa0 += d; xa1 += d; wa0 += d; wa1 += d; PIN(xa0); PIN(xa1); PIN(wa0); PIN(wa1);
    };
    BAR();
    STAMP_AT(1);
#pragma unroll
    for (int mb = 0; mb < 4; ++mb) xf[mb] = ld128(lds + xa0 + mb * 2048);
#pragma unroll
    for (int pnl = 0; pnl < 3; ++pnl) { wf[0][pnl][0] = ldtr(lds + wa0 + pnl * 8192, 512); wf[0][pnl][1] = ldtr(lds + wa1 + pnl * 8192, 512); }
    SB0();
    int st = 0;
    for (int t = 0; t < 16 - K1_D; ++t) { step(0, true, true); advance(st); step(1, true, true); }
    for (int t = 16 - K1_D; t < 16; ++t) { step(0, false, false); advance(st); step(1, false, false); }
  }
  STAMP_AT(2);
  const int lane = fresh_lane(), m = lane & 15, g = lane >> 4;
  const brs_t rws = mk_brs(P.ws, WS_BYTES);
  const int head = 2 * hp + wc, trow = wr * 64;
  const LAS float* stt = (const LAS float*)(lds + L_STATS);
  const LAS float* csl = (const LAS float*)(lds + L_CS1);
  const u32x4 ones4 = ones_bf16();
  f32x4 kvr[6];
#pragma unroll
  for (int i = 0; i < 6; ++i) kvr[i] = (f32x4){0.f, 0.f, 0.f, 0.f};
  float ck[2], cv[2];
#pragma unroll
  for (int nb = 0; nb < 2; ++nb) { ck[nb] = csl[64 + wc * 32 + nb * 16 + m]; cv[nb] = csl[128 + wc * 32 + nb * 16 + m]; }
#pragma unroll
  for (int mp = 0; mp < 2; ++mp) {
    u32x4 pkf[2], vf[2];
#pragma unroll
    for (int hb = 0; hb < 2; ++hb) {
      const int mb = 2 * mp + hb;
      const f32x4 mean = *(const LAS f32x4*)(stt + trow + mb * 16 + 4 * g), rstd = *(const LAS f32x4*)(stt + 128 + trow + mb * 16 + 4 * g), rs2 = *(const LAS f32x4*)(stt + 256 + trow + mb * 16 + 4 * g);
#pragma unroll
      for (int nb = 0; nb < 2; ++nb) {
        const f32x4 uk = acc[1][mb][nb] - mean * ck[nb], vv = (acc[2][mb][nb] - mean * cv[nb]) * rstd;
        pkf[nb][2 * hb] = phi_ln2(lo2(uk), lo2(rstd), lo2(rs2)); pkf[nb][2 * hb + 1] = phi_ln2(hi2(uk), hi2(rstd), hi2(rs2));
        vf[nb][2 * hb] = pk2(vv[0], vv[1]); vf[nb][2 * hb + 1] = pk2(vv[2], vv[3]);
      }
      SB0();
    }
#pragma unroll
    for (int a = 0; a < 2; ++a) {
#pragma unroll
      for (int b = 0; b < 2; ++b) kvr[a * 3 + b] = mfma(__builtin_bit_cast(bf16x8, pkf[a]), __builtin_bit_cast(bf16x8, vf[b]), kvr[a * 3 + b]);
      kvr[a * 3 + 2] = mfma(__builtin_bit_cast(bf16x8, pkf[a]), __builtin_bit_cast(bf16x8, ones4), kvr[a * 3 + 2]);
    }
  }
  f32x4 own[3];
#pragma unroll
  for (int i = 0; i < 3; ++i) {
    f32x4 snd;
#pragma unroll
    for (int e = 0; e < 4; ++e) { snd[e] = wr ? kvr[i][e] : kvr[3 + i][e]; own[i][e] = wr ? kvr[3 + i][e] : kvr[i][e]; }
    *(LAS f32x4*)(lds + K1_RED + wid * 3072 + i * 1024 + lane * 16) = snd;
  }
  WAITL0();
  BAR();
#pragma unroll
  for (int i = 0; i < 3; ++i) own[i] += *(LAS f32x4*)(lds + K1_RED + (wid ^ 2) * 3072 + i * 1024 + lane * 16);
  { const unsigned dst = WS_KVP + l * (512 * 2176) + (rt * 32 + head) * 2176;
    const u32x4 o = {pk2(own[0][0], own[0][1]), pk2(own[0][2], own[0][3]), pk2(own[1][0], own[1][1]), pk2(own[1][2], own[1][3])};
    stc16(rws, lane * 16, dst + wr * 1024, o);
    const u32x2 oz = {pk2(own[2][0], own[2][1]), pk2(own[2][2], own[2][3])};
    if (m == 0) stc8(rws, g * 8, dst + 2048 + wr * 32, oz); }
  { const int q = (lane >> 2) & 3, p = lane & 3;
    const int xa0 = (wr * 64 + m) * 128 + ((g ^ (m & 6)) * 16), xa1 = xa0 ^ 64;
    const int wa0 = 16384 + (8 * g + q) * 128 + (((4 * wc + (p >> 1)) ^ (2 * (((q >> 1) & 1) | ((g & 1) << 1)))) * 16) + (p & 1) * 8, wa1 = wa0 ^ 32;
    bf16x8 xg[2][2][4], wg[2][2][2];
    auto fetchq = [&](int set, int st) {
      LAS char* b = lds + st * K1_STAGE;
#pragma unroll
      for (int s = 0; s < 2; ++s) {
        wg[set][s][0] = ldtr(b + wa0 + s * 4096, 512); wg[set][s][1] = ldtr(b + wa1 + s * 4096, 512);
#pragma unroll
        for (int mb = 0; mb < 4; ++mb) xg[set][s][mb] = ld128(b + (s ? xa1 : xa0) + mb * 2048);
      }
    };
    auto mmaq = [&](int set) {
#pragma unroll
      for (int s = 0; s < 2; ++s)
#pragma unroll
        for (int mb = 0; mb < 4; ++mb)
#pragma unroll
          for (int nb = 0; nb < 2; ++nb) acc[0][mb][nb] = mfma(wg[set][s][nb], xg[set][s][mb], acc[0][mb][nb]);
    };
#define K1Q_MIX() do { _Pragma("unroll") for (int i_ = 0; i_ < 8; ++i_) { SGB(0x8, 1); SGB(0x100, 2); } SGB(0x8, 8); SB0(); } while (0)
    fetchq(0, 16 % 3);
    SB0();
    c_signal<true>(P.ws, 128 * l + C_KV + hp, lds, 5 * l + 0, 4);
#pragma unroll
    for (int u = 1; u < K1_D; ++u) { WAITL0(); BAR(); fetchq(u & 1, (16 + u) % 3); mmaq((u & 1) ^ 1); K1Q_MIX(); }
    mmaq((K1_D - 1) & 1); }
  f32x4 c4[2];
#pragma unroll
  for (int nb = 0; nb < 2; ++nb) c4[nb] = *(const LAS f32x4*)(csl + wc * 32 + nb * 16 + 4 * g);
  u32x4 pq[4];
#pragma unroll
  for (int mb = 0; mb < 4; ++mb) {
    const f32x2 mr = {stt[trow + mb * 16 + m], stt[128 + trow + mb * 16 + m]};
    const float rs2 = stt[256 + trow + mb * 16 + m];
#pragma unroll
    for (int nb = 0; nb < 2; ++nb) {
      const f32x4 uq = acc[0][mb][nb] - mr[0] * c4[nb];
      const f32x2 r2 = {mr[1], mr[1]}, s2 = {rs2, rs2};
      pq[mb][2 * nb] = phi_ln2(lo2(uq), r2, s2); pq[mb][2 * nb + 1] = phi_ln2(hi2(uq), r2, s2);
    }
    PIN(pq[mb]);
  }
  STAMP_END(1 + 5 * l);
  STAMP_AT(0);
  BAR();
  STAMP_AT(1); STAMP_AT(2);
  { LAS char* kb = lds + L_KV + wc * 2112;
    const bf16x8 k0 = ld128(kb + lane * 16), k1 = ld128(kb + 1024 + lane * 16), zf = ld128(kb + 2048 + g * 16);
    const f32x4 z4 = {0.f, 0.f, 0.f, 0.f};
#pragma unroll
    for (int mb = 0; mb < 4; ++mb) {
      const bf16x8 x = __builtin_bit_cast(bf16x8, pq[mb]);
      const f32x4 dn = mfma(zf, x, z4), d0 = mfma(k0, x, z4), d1 = mfma(k1, x, z4);
      const float rr = __builtin_amdgcn_rcpf(dn[0] + 1e-6f);
      const u32x4 o = {pk2(d0[0] * rr, d0[1] * rr), pk2(d0[2] * rr, d0[3] * rr), pk2(d1[0] * rr, d1[1] * rr), pk2(d1[2] * rr, d1[3] * rr)};
      stc16(rws, lane * 16, WS_PQ + ((rt * 8 + wr * 4 + mb) * 32 + head) * 1024, o);
    } }
  BAR();
  c_signal<true>(P.ws, 128 * l + C_O + rt, lds, 5 * l + 1, 4);
  STAMP_END(2 + 5 * l);
}

constexpr int K2_NT = 16, K2_STAGE = 24576, K2_NS = 6;
template <bool P1B> DI void p2_load(const Params& P, int l, LAS char* lds, int j) {
  const int lane = fresh_lane();
  const int bid = fresh_s(blockIdx.x), xcd = bid & 7, loc = bid >> 3;
  const int rt = (xcd & 3) * 4 + (loc & 3), ct = (xcd >> 2) * 8 + (loc >> 2);
  const int rot = ROT_K2 ? ((loc >> 2) + 2 * (loc & 3)) & 7 : 0;
  const rsrc_t rA = mk_rsrc(P.ws, WS_BYTES), rW = mk_rsrc(P.wo, D_ * D_ * 2);
  const int r = lane >> 3, sl8 = lane & 7, f = ((r >> 1) & 1) | ((j & 1) << 1);
  const unsigned voffW = r * 2048 + ((sl8 ^ (2 * f)) * 16), voffL = lane * 16;
  auto stX = [&](int t, int sl) {
    const unsigned sa = WS_PQ + (unsigned)((rt * 8 + 2 * j) * 32768 + ((t & ~7) | ((t + rot) & 7)) * 2048);
#pragma unroll
    for (int u = 0; u < 4; ++u) dma16c(rA, lds + sl * K2_STAGE + (4 * j + u) * 1024, voffL, sa + (u >> 1) * 32768 + (u & 1) * 1024);
  };
  auto stW = [&](int t, int sl) {
    const unsigned sw = (unsigned)((((t & ~7) | ((t + rot) & 7)) * 64 + j * 8) * 2048 + ct * 128);
#pragma unroll
    for (int u = 0; u < 2; ++u) dma16(rW, lds + sl * K2_STAGE + 16384 + (j + 4 * u) * 1024, voffW, sw + u * 32 * 2048);
  };
#pragma unroll
  for (int t = 0; t < 5; ++t) stW(t, t);
  l_wait(P.ws, 128 * l + C_O + rt, 16, lds, 5 * l + 2, j);
#pragma unroll
  for (int t = 0; t < 5; ++t) stX(t, t);
  WAITV(16); BAR(); stX(5, 5); stW(5, 5);
  WAITV(18); BAR(); stX(6, 0); stW(6, 0);
  WAITV(20); BAR(); stX(7, 1); stW(7, 1);
  WAITV(22); BAR(); stX(8, 2); stW(8, 2);
  int sl = 3;
  for (int t = 4; t < K2_NT - 5; ++t) {
    WAITV(24); BAR();
    stX(t + 5, sl); stW(t + 5, sl);
    sl = sl == K2_NS - 1 ? 0 : sl + 1;
  }
  drain_bar<4, 6>();
  BAR();
}
DI void p2_comp(const Params& P, int l, LAS char* lds, int wid) {
  STAMP_DECL STAMP_AT(0);
  if (wid >= 4) {
#pragma unroll 1
    for (int i = 0; i < K2_NT + 1; ++i) BAR();
    return;
  }
  const int lane = fresh_lane();
  const int bid = fresh_s(blockIdx.x), xcd = bid & 7, loc = bid >> 3;
  const int rt = (xcd & 3) * 4 + (loc & 3), ct = (xcd >> 2) * 8 + (loc >> 2);
  const int m = lane & 15, g = lane >> 4, q = (lane >> 2) & 3, p = lane & 3;
  const int row0 = rt * 128 + wid * 32 + m;
  const unsigned off0 = (unsigned)(row0 * D_ + ct * 64 + 8 * g) * 2;
  int wa[4];
  { const int f = ((q >> 1) & 1) | ((g & 1) << 1);
#pragma unroll
    for (int nb = 0; nb < 4; ++nb) wa[nb] = 16384 + (8 * g + q) * 128 + (((4 * (nb >> 1) + p) ^ (2 * f)) * 16) + (((nb & 1) ^ (p >> 1)) * 8); }
  const int xa = wid * 4096 + lane * 16;
  f32x4 acc[2][4];
  { const brs_t rxi = mk_brs(l ? P.out : P.x0, S_ * D_ * 2);
    u32x4 res[2][2];
#pragma unroll
    for (int mb = 0; mb < 2; ++mb) { res[mb][0] = ldc16(rxi, off0, mb * 32768); res[mb][1] = ldc16(rxi, off0, mb * 32768 + 64); }
#pragma unroll
    for (int mb = 0; mb < 2; ++mb) res_unpack(acc[mb], res[mb], g >= 2, false); }
  bf16x8 xf[2][2][2], wf[2][2][4];
  auto fetch = [&](int set, int sl) {
    LAS char* b = lds + sl * K2_STAGE;
#pragma unroll
    for (int s = 0; s < 2; ++s) {
      xf[set][s][0] = ld128(b + xa + s * 1024); xf[set][s][1] = ld128(b + xa + s * 1024 + 2048);
#pragma unroll
      for (int nb = 0; nb < 4; ++nb) wf[set][s][nb] = ldtr(b + wa[nb] + s * 4096, 512);
    }
  };
  auto mma = [&](int set) {
#pragma unroll
    for (int s = 0; s < 2; ++s)
#pragma unroll
      for (int nb = 0; nb < 4; ++nb)
#pragma unroll
        for (int mb = 0; mb < 2; ++mb) acc[mb][nb] = mfma(wf[set][s][nb], xf[set][s][mb], acc[mb][nb]);
  };
#define K2_MIX() do { _Pragma("unroll") for (int i_ = 0; i_ < 10; ++i_) { SGB(0x8, 1); SGB(0x100, 2); } SGB(0x8, 6); SB0(); } while (0)
  BAR();
  STAMP_AT(1);
  fetch(0, 0);
  SB0();
  int sl = 1;
  for (int t = 1; t < K2_NT - 1; t += 2) {
    WAITL0(); BAR();
    fetch(1, sl); mma(0); K2_MIX();
    sl = sl == K2_NS - 1 ? 0 : sl + 1;
    WAITL0(); BAR();
    fetch(0, sl); mma(1); K2_MIX();
    sl = sl == K2_NS - 1 ? 0 : sl + 1;
  }
  WAITL0(); BAR();
  fetch(1, sl); mma(0); K2_MIX();
  mma(1);
  STAMP_AT(2);
  const brs_t rws = mk_brs(P.ws, WS_BYTES);
#pragma unroll
  for (int mb = 0; mb < 2; ++mb)
    x_store(acc[mb], rws, WS_X1 + off0 + xs_adj(m) + mb * 32768, rws, WS_ST + (unsigned)(2 * l + 1) * 262144u + (ct * 2048 + row0 + mb * 16) * 8, m, g, true);
  BAR();
  c_signal<true>(P.ws, 128 * l + C_X1 + (rt >> 1), lds, 5 * l + 2, 4, (ct >> 1) == (l ? 0 : rt >> 1) ? 128 * l + F_X1 + (rt >> 1) : -1);
  STAMP_END(3 + 5 * l);
}

DI void p3_load(const Params& P, int l, LAS char* lds, int j) {
  const int lane = fresh_lane(), tid = j * 64 + lane;
  const int bid = fresh_s(blockIdx.x), xcd = bid & 7, loc = bid >> 3;
  const int rt = (xcd & 1) * 4 + (loc & 3), ct = (xcd >> 1) * 8 + (loc >> 2);
  const int rot = l ? 0 : 2 * rt;
  const int mt = j & 1, hi2 = j >> 1;
  unsigned voffA, voffB;
  { const int r = lane >> 3, sl8 = lane & 7; voffA = r * 2048 + ((sl8 ^ (r & 6)) * 16);
    const int qq = lane >> 4, sl = lane & 15, f = qq | (hi2 << 2), ch = sl ^ (2 * f) ^ mt; voffB = ch < 14 ? (unsigned)(qq * 7168 + ch * 16) : OOB_V; }
  const rsrc_t rS = mk_rsrc(P.ws, WS_BYTES), rW = mk_rsrc(mt ? P.wu : P.wg, D_ * F_ * 2);
  auto stA = [&](int half, int t) {
    const unsigned sb = WS_X1 + (unsigned)((rt * 256 + half * 128 + j * 32) * 2048 + ((t + rot) & 15) * 128);
#pragma unroll
    for (int u = 0; u < 4; ++u) dma16c(rS, lds + half * 32768 + (t & 1) * 16384 + (4 * j + u) * 1024, voffA, sb + u * 16384);
  };
  auto stB = [&](int s, int t) {
    const unsigned sb = (unsigned)((((t + rot) & 15) * 64 + s * 32 + hi2 * 8) * 7168 + ct * 224);
#pragma unroll
    for (int u = 0; u < 4; ++u) { const int i = (u & 1) + 4 * (u >> 1);
      dma16(rW, lds + 65536 + ((t & 1) * 2 + s) * 16384 + mt * 8192 + (i + 2 * hi2) * 1024, voffB, sb + i * 4 * 7168); }
  };
  stB(0, 0); stB(1, 0); stB(0, 1);
  l_wait(P.ws, 128 * l + F_X1 + rt, 4, lds, 5 * l + 3, j);
  stA(0, 0); stA(1, 0); stA(0, 1);
  int ca[4]; f32x4 cs[4]; bf16x8 cf[4];
  { const int g = lane >> 4, q = (lane >> 2) & 3, p = lane & 3, f2 = 2 * (q | ((g & 1) << 2));
#pragma unroll
    for (int u = 0; u < 4; ++u) { const int blk = min(j + 4 * u, 13), mm = blk >= 7; ca[u] = 65536 + mm * 8192 + (8 * g + q) * 256 + (((2 * (blk - 7 * mm) + (p >> 1)) ^ f2 ^ mm) * 16) + (p & 1) * 8;
      cs[u] = (f32x4){0.f, 0.f, 0.f, 0.f}; } }
  const bf16x8 ones = __builtin_bit_cast(bf16x8, ones_bf16());
  auto csr = [&](int piece) { if (l == 0) {
#pragma unroll
      for (int u = 0; u < 4; ++u) cf[u] = ldtr(lds + ca[u] + piece * 16384, 1024); } };
  auto csd = [&]() { if (l == 0) {
#pragma unroll
      for (int u = 0; u < 4; ++u) { cs[u] = mfma(ones, cf[u], cs[u]); PIN(cs[u]); } } };
  WAITV(8); BAR();
  stA(1, 1); csr(0); WAITV(8); BAR();
  unsigned look = 0;
  if (j == 0) LOOK(look, rep_ptr(P.ws, 128 * l + C_X1 + rt));
  stB(1, 1); csd(); BAR();
  stB(0, 2); csr(1);
  if (j == 0) LOOK_CHECK(look, rep_ptr(P.ws, 128 * l + C_X1 + rt), 32u, 8);
  BAR();
  stA(0, 2); csd(); WAITV(16);
  u32x2 sv[16];
  { const unsigned so = WS_ST + (unsigned)(2 * l + 1) * 262144u + rt * 2048, vo = tid * 8;
#pragma unroll
    for (int i = 0; i < 16; ++i) ALOAD8C(sv[i], rS, vo, so + i * 16384); }
  BAR();
  stA(1, 2); csr(2); WAITV(32); BAR();
  stB(1, 2); csd(); WAITV(32); BAR();
  stB(0, 3); csr(3); WAITV(32); BAR();
  csd();
  if (l == 0) { const brs_t rws = mk_brs(P.ws, WS_BYTES);
#pragma unroll
    for (int u = 0; u < 4; ++u) if (lane < 16 && j + 4 * u < 14) __builtin_amdgcn_raw_buffer_store_b32(__float_as_uint(cs[u][0]), rws, ((j + 4 * u) * 16 + lane) * 4, WS_CS + (16 * 3072 + rt * 7168 + ct * 224) * 4, 16); }
  FENCE();
  stA(0, 3); WAITV(16); BAR();
  stA(1, 3);
#pragma unroll
  for (int i = 0; i < 16; ++i) PIN(sv[i]);
  *(LAS f32x2*)(lds + L_STATS + tid * 8) = combine_stats<16, false>(sv);
  WAITL0();
  WAITV(16); BAR();
  stB(1, 3); WAITV(16); BAR();
  stB(0, 4); WAITV(16); BAR();
  stA(0, 4); WAITV(16);
  if (l == 0) {
    unsigned old = 0;
    if (lane == 0) old = __hip_atomic_fetch_add((LAS unsigned*)(lds + L_SYNC + 68), 1u, __ATOMIC_RELAXED, __HIP_MEMORY_SCOPE_WORKGROUP);
    if (__builtin_amdgcn_readfirstlane(old) == 3 && lane == 0) __hip_atomic_fetch_add(cnt_ptr(P.ws, ct), 1u, __ATOMIC_RELAXED, __HIP_MEMORY_SCOPE_AGENT);
  }
  BAR();
  for (int t = 3; t < 14; ++t) {
    const char* pfp = (const char*)P.wd + ((bid * 4 + j) * 56 + (t - 3) * 7) * 128;
    const bool pf = l == 0 && t < 11;
    stA(1, t + 1); if (pf) pf_lines<2>(pfp); WAITV(16); BAR();
    stB(1, t + 1); if (pf) pf_lines<2>(pfp + 256); WAITV(16); BAR();
    stB(0, t + 2); if (pf) pf_lines<2>(pfp + 512); WAITV(16); BAR();
    stA(0, t + 2); if (pf) pf_lines<1>(pfp + 768); WAITV(16); BAR();
  }
  stA(1, 15); WAITV(16); BAR();
  stB(1, 15); WAITV(16); BAR();
  WAITV(12); BAR();
  WAITV(8); BAR();
  WAITV(4); BAR();
  WAITV(0);
  if (l == 0 && j == 0) poll_ge(cnt_ptr(P.ws, ct), 8);
  BAR();
  float cv[8];
  if (l == 0) { const brs_t rws = mk_brs(P.ws, WS_BYTES);
#pragma unroll
    for (int i = 0; i < 8; ++i) cv[i] = __uint_as_float(__builtin_amdgcn_raw_buffer_load_b32(rws, min(tid, 223) * 4, WS_CS + (16 * 3072 + i * 7168 + ct * 224) * 4, 16)); }
  BAR();
  if (l == 0) { const float c = ((cv[0] + cv[1]) + (cv[2] + cv[3])) + ((cv[4] + cv[5]) + (cv[6] + cv[7])); if (tid < 224) *(LAS float*)(lds + L_CS + tid * 4) = c; WAITL0(); }
  BAR();
  BAR();
}
DI void p3_comp(const Params& P, int l, LAS char* lds, int wid) {
  STAMP_DECL STAMP_AT(0);
  const int bid = fresh_s(blockIdx.x), xcd = bid & 7, loc = bid >> 3;
  const int rt = (xcd & 1) * 4 + (loc & 3), ct = (xcd >> 1) * 8 + (loc >> 2);
  const int grp = wid >> 2, wrl = (wid >> 1) & 1, wc = wid & 1;
  f32x4 acc[4][7];
  {
    const int lane = fresh_lane();
    const int m = lane & 15, g = lane >> 4, q = (lane >> 2) & 3, p = lane & 3;
    int xa[2], wa[7];
#pragma unroll
    for (int s = 0; s < 2; ++s) xa[s] = grp * 32768 + (wrl * 64 + m) * 128 + (((4 * s + g) ^ (m & 6)) * 16);
    { const int f2 = 2 * (q | ((g & 1) << 2)), ro = 65536 + (8 * g + q) * 256, c0 = 7 * wc;
#pragma unroll
      for (int mm = 0; mm < 2; ++mm) {
#pragma unroll
        for (int b = 0; b < 2; ++b) wa[3 * mm + b] = ro + mm * 8192 + (((c0 + p) ^ f2 ^ mm) * 16) + ((b ^ (p >> 1)) * 8);
        wa[3 * mm + 2] = ro + mm * 8192 + (((c0 + 4 + (p >> 1)) ^ f2 ^ mm) * 16) + (p & 1) * 8;
      }
      wa[6] = ro + (p >> 1) * 8192 + (((c0 + 6) ^ f2 ^ (p >> 1)) * 16) + (p & 1) * 8; }
#pragma unroll
    for (int a = 0; a < 4; ++a)
#pragma unroll
      for (int b = 0; b < 7; ++b) acc[a][b] = (f32x4){0.f, 0.f, 0.f, 0.f};
    BAR();
    STAMP_AT(1);
    if (grp == 1) BAR();
    for (int t2 = 0; t2 < 16; t2 += 2) {
#pragma unroll
      for (int buf = 0; buf < 2; ++buf) {
#pragma unroll
        for (int s = 0; s < 2; ++s) {
          bf16x8 xf[4], wf[7];
#pragma unroll
          for (int mb = 0; mb < 4; ++mb) xf[mb] = ld128(lds + xa[s] + buf * 16384 + mb * 2048);
#pragma unroll
          for (int T = 0; T < 7; ++T) wf[T] = ldtr(lds + wa[T] + (buf * 2 + s) * 16384, 1024);
          WAITL0();
          BAR();
          __builtin_amdgcn_s_setprio(1);
#pragma unroll
          for (int mb = 0; mb < 4; ++mb)
#pragma unroll
            for (int T0 = 0; T0 < 7; ++T0) { const int T = (SNAKE && (mb & 1)) ? 6 - T0 : T0; acc[mb][T] = mfma(wf[T], xf[mb], acc[mb][T]); }
          __builtin_amdgcn_s_setprio(0);
          if (buf == 0 || s == 0 || t2 < 14 || grp == 0) BAR();
        }
      }
    }
  }
  STAMP_AT(2);
  const int tz = fresh_lane();
  const int m = tz & 15, g = tz >> 4;
  const LAS f32x2* stt = (const LAS f32x2*)(lds + L_STATS);
  const LAS float* csl = (const LAS float*)(lds + L_CS);
  f32x4 c4[7];
#pragma unroll
  for (int mm = 0; mm < 2; ++mm) {
#pragma unroll
    for (int b = 0; b < 2; ++b) c4[3 * mm + b] = *(const LAS f32x4*)(csl + mm * 112 + wc * 56 + 8 * g + 4 * (b ^ (g >> 1)));
    c4[3 * mm + 2] = *(const LAS f32x4*)(csl + mm * 112 + wc * 56 + 32 + 4 * g);
  }
  c4[6] = *(const LAS f32x4*)(csl + (g >> 1) * 112 + wc * 56 + 48 + 4 * (g & 1));
  const bool hi = g >= 2;
  const brs_t rws = mk_brs(P.ws, WS_BYTES);
  const unsigned hb = WS_H + (unsigned)(rt * 16 + grp * 8 + wrl * 4) * 114688u + (unsigned)(7 * (2 * ct + wc)) * 256u;
  const unsigned v16 = m * 16 + g * 256, v8 = m * 16 + (g >> 1) * 256 + (g & 1) * 114688, vz = m * 16 + ((g >> 1) + 2 * (g & 1)) * 114688;
  f32x4 zv[4];
  u32x2 h2[4];
#pragma unroll
  for (int mb = 0; mb < 4; ++mb) {
    const f32x2 mr = stt[grp * 128 + wrl * 64 + mb * 16 + m];
    const float r2 = mr[1] * mr[1];
    u32x2 hv[3];
#pragma unroll
    for (int b = 0; b < 3; ++b) {
      const f32x4 hh = ((acc[mb][b] - mr[0] * c4[b]) * r2) * (acc[mb][3 + b] - mr[0] * c4[3 + b]);
      hv[b][0] = pk2(hh[0], hh[1]); hv[b][1] = pk2(hh[2], hh[3]);
    }
    zv[mb] = (acc[mb][6] - mr[0] * c4[6]) * mr[1];
    const u32x4 o = {hi ? hv[1][0] : hv[0][0], hi ? hv[1][1] : hv[0][1], hi ? hv[0][0] : hv[1][0], hi ? hv[0][1] : hv[1][1]};
    stc16(rws, v16, hb + mb * 114688, o);
    h2[mb] = hv[2];
    if (mb & 1) {
      const auto s0 = __builtin_amdgcn_permlane16_swap(h2[mb - 1][0], h2[mb][0], false, false), s1 = __builtin_amdgcn_permlane16_swap(h2[mb - 1][1], h2[mb][1], false, false);
      const u32x4 o2 = {s0[0], s1[0], s0[1], s1[1]};
      stc16(rws, v8, hb + (mb - 1) * 114688 + 1024, o2);
    }
  }
  { u32x2 oz[2];
#pragma unroll
    for (int mp = 0; mp < 2; ++mp) {
      float hh[4];
#pragma unroll
      for (int e = 0; e < 4; ++e) {
        const auto r = __builtin_amdgcn_permlane32_swap(__float_as_uint(zv[2 * mp][e]), __float_as_uint(zv[2 * mp + 1][e]), false, false);
        hh[e] = __uint_as_float(r[0]) * __uint_as_float(r[1]);
      }
      oz[mp][0] = pk2(hh[0], hh[1]); oz[mp][1] = pk2(hh[2], hh[3]);
    }
    const auto s0 = __builtin_amdgcn_permlane16_swap(oz[0][0], oz[1][0], false, false), s1 = __builtin_amdgcn_permlane16_swap(oz[0][1], oz[1][1], false, false);
    const u32x4 o2 = {s0[0], s1[0], s0[1], s1[1]};
    stc16(rws, vz, hb + 1536, o2); }
  BAR();
  c_signal<true>(P.ws, 128 * l + C_H + rt * 2 + (ct >> 4), lds, 5 * l + 3, 8, (ct & 15) < 4 ? 128 * l + F_H + rt * 2 + (ct >> 4) : -1);
  STAMP_END(4 + 5 * l);
}

constexpr int K4_NT = 28, K4_STAGE = 32768, K4_NS = 4;
DI void p4_load(const Params& P, int l, LAS char* lds, int j) {
  const int lane = fresh_lane();
  const int bid = fresh_s(blockIdx.x), xcd = bid & 7, loc = bid >> 3;
  const int kh = xcd & 1, rt = (xcd >> 1) * 4 + (loc & 3), ct = loc >> 2;
  const int rot = ROT_K4 ? (ct + 2 * (loc & 3)) % 7 : 0;
  const rsrc_t rA = mk_rsrc(P.ws, WS_BYTES), rW = mk_rsrc(P.wd, F_ * D_ * 2);
  const int r = lane >> 3, sl8 = lane & 7, f = ((r >> 1) & 1) | ((j & 1) << 1);
  const unsigned voffW = r * 2048 + ((sl8 ^ (2 * f)) * 16), voffL = lane * 16;
  auto stX = [&](int kt, int sl) {
    const unsigned sa = WS_H + (unsigned)((rt * 8 + 2 * j) * 114688 + kt * 2048);
#pragma unroll
    for (int u = 0; u < 4; ++u) dma16c(rA, lds + sl * K4_STAGE + (4 * j + u) * 1024, voffL, sa + (u >> 1) * 114688 + (u & 1) * 1024);
  };
  auto stW = [&](int kt, int sl) {
    const unsigned sw = (unsigned)((kt * 64 + j * 8) * 2048 + ct * 256);
#pragma unroll
    for (int u = 0; u < 4; ++u) dma16(rW, lds + sl * K4_STAGE + 16384 + (u >> 1) * 8192 + (j + 4 * (u & 1)) * 1024, voffW, sw + (u >> 1) * 128 + (u & 1) * 32 * 2048);
  };
  const int k0 = kh * K4_NT + rot, k1 = kh * K4_NT + (rot + 1) % 7, k2 = kh * K4_NT + (rot + 2) % 7;
  stW(k0, 0); stW(k1, 1); stW(k2, 2);
  l_wait(P.ws, 128 * l + F_H + (rt >> 1) * 2 + kh, 4, lds, 5 * l + 4, j);
  stX(k0, 0); stX(k1, 1); stX(k2, 2);
  int w0 = kh * K4_NT, wr_ = (rot + 3) % 7, sl = 3;
  auto next = [&]() { const int kt = w0 + wr_; stX(kt, sl); stW(kt, sl); wr_ = wr_ == 6 ? 0 : wr_ + 1; if (wr_ == rot) w0 += 7; sl = (sl + 1) & 3; };
  WAITV(8); BAR(); next();
  WAITV(12); BAR(); next();
  WAITV(16); BAR();
  unsigned look = 0;
  if (j == 0) LOOK(look, rep_ptr(P.ws, 128 * l + C_H + (rt >> 1) * 2 + kh));
  next();
  WAITV(16); BAR(); next();
  if (j == 0) LOOK_CHECK(look, rep_ptr(P.ws, 128 * l + C_H + (rt >> 1) * 2 + kh), 16u, 16);
  for (int t = 4; t < K4_NT - 3; ++t) { WAITV(16); BAR(); next(); }
  drain_bar<2, 8>();
  BAR();
}
DI void p4_comp(const Params& P, int l, LAS char* lds, int wid) {
  STAMP_DECL STAMP_AT(0);
  if (wid >= 4) {
#pragma unroll 1
    for (int i = 0; i < K4_NT + 1; ++i) BAR();
    return;
  }
  const int lane = fresh_lane();
  const int bid = fresh_s(blockIdx.x), xcd = bid & 7, loc = bid >> 3;
  const int kh = xcd & 1, rt = (xcd >> 1) * 4 + (loc & 3), ct = loc >> 2;
  const int wr = wid >> 1, wc = wid & 1;
  const int m = lane & 15, g = lane >> 4, q = (lane >> 2) & 3, p = lane & 3;
  const int row0 = rt * 128 + wr * 64 + kh * 32 + m;
  const unsigned off0 = (unsigned)(row0 * D_ + ct * 128 + wc * 64 + 8 * g) * 2;
  int wa[4];
  { const int f = ((q >> 1) & 1) | ((g & 1) << 1);
#pragma unroll
    for (int nb = 0; nb < 4; ++nb) wa[nb] = 16384 + wc * 8192 + (8 * g + q) * 128 + (((4 * (nb >> 1) + p) ^ (2 * f)) * 16) + (((nb & 1) ^ (p >> 1)) * 8); }
  const int xa0 = wr * 8192 + lane * 16 + kh * 4096, xa1 = wr * 8192 + lane * 16 + 4096 - kh * 4096;
  f32x4 acc[4][4];
#pragma unroll
  for (int a = 0; a < 4; ++a)
#pragma unroll
    for (int b = 0; b < 4; ++b) acc[a][b] = (f32x4){0.f, 0.f, 0.f, 0.f};
  bf16x8 xf[2][4], wf[2][4];
  auto fetch = [&](int s, int sl) {
    LAS char* b = lds + sl * K4_STAGE;
    xf[s][0] = ld128(b + xa0 + s * 1024); xf[s][1] = ld128(b + xa0 + s * 1024 + 2048);
    xf[s][2] = ld128(b + xa1 + s * 1024); xf[s][3] = ld128(b + xa1 + s * 1024 + 2048);
#pragma unroll
    for (int nb = 0; nb < 4; ++nb) wf[s][nb] = ldtr(b + wa[nb] + s * 4096, 512);
  };
  auto mma = [&](int s) {
#pragma unroll
    for (int nb = 0; nb < 4; ++nb)
#pragma unroll
      for (int mb = 0; mb < 4; ++mb) acc[mb][nb] = mfma(wf[s][nb], xf[s][mb], acc[mb][nb]);
  };
#define K4_MIX() do { _Pragma("unroll") for (int i_ = 0; i_ < 12; ++i_) { SGB(0x8, 1); SGB(0x100, 1); } SGB(0x8, 4); SB0(); } while (0)
  const brs_t rws = mk_brs(P.ws, WS_BYTES);
  BAR();
  STAMP_AT(1);
  fetch(0, 0);
  SB0();
  int sl = 0;
  for (int t = 0; t < K4_NT - 1; ++t) {
    fetch(1, sl); mma(0); K4_MIX();
    sl = (sl + 1) & (K4_NS - 1);
    WAITL0(); BAR();
    fetch(0, sl); mma(1); K4_MIX();
  }
  fetch(1, sl); mma(0); K4_MIX();
  mma(1);
  STAMP_AT(2);
  const int pair = rt * 8 + ct;
  u32x4 res[2][2];
#pragma unroll
  for (int mb = 0; mb < 2; ++mb) { res[mb][0] = ldc16(rws, off0, WS_X1 + mb * 32768); res[mb][1] = ldc16(rws, off0, WS_X1 + mb * 32768 + 64); }
  FENCE();
#pragma unroll
  for (int i = 0; i < 4; ++i) { const f32x4 a = acc[2 + (i >> 1)][2 * (i & 1)], b = acc[2 + (i >> 1)][2 * (i & 1) + 1];
    const u32x4 o = {pk2(a[0], a[1]), pk2(a[2], a[3]), pk2(b[0], b[1]), pk2(b[2], b[3])};
    stc16(rws, lane * 16, WS_XCH + pair * 65536 + ((kh ^ 1) * 4 + wid) * 8192 + i * 1024, o); }
#pragma unroll
  for (int mb = 0; mb < 2; ++mb) { res_unpack(acc[mb], res[mb], g >= 2, true); PIN(acc[mb][0]); PIN(acc[mb][1]); PIN(acc[mb][2]); PIN(acc[mb][3]); }
  WAITV(0);
  volatile LAS unsigned* relay = (volatile LAS unsigned*)(lds + L_SYNC + 48);
  { unsigned old = 0;
    if (lane == 0) old = __hip_atomic_fetch_add((LAS unsigned*)(lds + L_SYNC + 52), 1u, __ATOMIC_RELAXED, __HIP_MEMORY_SCOPE_WORKGROUP);
    old = __builtin_amdgcn_readfirstlane(old);
    if ((old & 3) == 3) {
      if (lane == 0) __hip_atomic_store(cnt_ptr(P.ws, C_FLAG + 256 * l + pair * 2 + kh), 1u, __ATOMIC_RELAXED, __HIP_MEMORY_SCOPE_AGENT);
      poll_ge(cnt_ptr(P.ws, C_FLAG + 256 * l + pair * 2 + (kh ^ 1)), 1);
      *relay = l + 1;
    } else { unsigned spins = 0; while ((unsigned)__builtin_amdgcn_readfirstlane(*relay) < (unsigned)(l + 1) && ++spins < (1u << 24)) __builtin_amdgcn_s_sleep(1); }
    FENCE(); }
  u32x4 in[8];
#pragma unroll
  for (int i = 0; i < 4; ++i) in[i] = ldc16(rws, lane * 16, WS_XCH + pair * 65536 + (kh * 4 + wid) * 8192 + i * 1024);
  BAR();
#pragma unroll
  for (int i = 0; i < 4; ++i) { const f32x4 a = {bflo(in[i][0]), bfhi(in[i][0]), bflo(in[i][1]), bfhi(in[i][1])}, b = {bflo(in[i][2]), bfhi(in[i][2]), bflo(in[i][3]), bfhi(in[i][3])};
    acc[i >> 1][2 * (i & 1)] += a; acc[i >> 1][2 * (i & 1) + 1] += b; }
  const brs_t rxo = mk_brs(P.out, S_ * D_ * 2);
#pragma unroll
  for (int mb = 0; mb < 2; ++mb)
    x_store(acc[mb], rxo, off0 + xs_adj(m) + mb * 32768, rws, WS_ST + (unsigned)((2 * l + 2) & 3) * 262144u + ((ct * 2 + wc) * 2048 + row0 + mb * 16) * 8, m, g, l == 0);
  if (l == 0) c_signal<true>(P.ws, 128 + C_X + rt, lds, 4, 4, ct < 2 ? 128 + F_X + rt : -1);
  else if (bid == 0 && wid == 0) { const u64 tag = call_tag(P.ws); if (lane == 0) __hip_atomic_store((u64*)(P.ws + WS_EPOCH), tag, __ATOMIC_RELAXED, __HIP_MEMORY_SCOPE_AGENT); }
  STAMP_END(5 + 5 * l);
}

DI void init_sync(LAS char* lds) { if (threadIdx.x < 32) *(LAS unsigned*)(lds + L_SYNC + threadIdx.x * 4) = 0u; WAITL0(); }

__global__ __launch_bounds__(768, 3) void k_mega(Params P) {
  extern __shared__ __attribute__((aligned(16))) char smem[];
  LAS char* lds = (LAS char*)smem;
  const int wid = __builtin_amdgcn_readfirstlane(threadIdx.x >> 6);
  if (wid >= 8) {
    const int j = wid & 3;
#pragma unroll 1
    for (int l = 0; l < 2; ++l) { p1_load(fresh_params(), fresh_s(l), lds, fresh_s(j)); p2_load<true>(fresh_params(), fresh_s(l), lds, fresh_s(j)); p3_load(fresh_params(), fresh_s(l), lds, fresh_s(j)); p4_load(fresh_params(), fresh_s(l), lds, fresh_s(j)); }
  } else {
    init_sync(lds);
#pragma unroll 1
    for (int l = 0; l < 2; ++l) {
      p1_comp(fresh_params(), fresh_s(l), lds, fresh_s(wid)); p2_comp(fresh_params(), fresh_s(l), lds, fresh_s(wid));
      p3_comp(fresh_params(), fresh_s(l), lds, fresh_s(wid)); p4_comp(fresh_params(), fresh_s(l), lds, fresh_s(wid));
    }
  }
}

extern "C" void kernel_launch(void* const* d_in, const int* in_sizes, int n_in, void* d_out, int out_size, void* d_ws, size_t ws_size, hipStream_t stream) {
  Params P;
  P.x0 = (const u16*)d_in[0]; P.wqkv = (const u16*)d_in[1]; P.wo = (const u16*)d_in[2];
  P.wg = (const u16*)d_in[4]; P.wu = (const u16*)d_in[5]; P.wd = (const u16*)d_in[6];
  P.out = (u16*)d_out; P.ws = (char*)d_ws;
  (void)hipFuncSetAttribute((const void*)k_mega, hipFuncAttributeMaxDynamicSharedMemorySize, L_BYTES);
  k_mega<<<256, 768, L_BYTES, stream>>>(P);
}
```
